```python
import torch

def _softmax(x, axis=-1):
    return torch.softmax(x, dim=axis)
def _gelu(x):
    return torch.nn.functional.gelu(x, approximate="tanh")
def _silu(x):
    return torch.nn.functional.silu(x)
def _sigmoid(x):
    return torch.sigmoid(x)
def _layernorm(x, axis=-1, eps=1e-5):
    m = x.mean(dim=axis, keepdim=True)
    v = x.var(dim=axis, keepdim=True, unbiased=False)
    return (x - m) / torch.sqrt(v + eps)
def _scatter_add(tgt, idx, src, axis=0):
    return tgt.clone().index_add_(axis, idx, src)
def _segment_sum(src, seg, n):
    out = torch.zeros((n,) + src.shape[1:], dtype=src.dtype)
    return out.index_add_(0, seg, src)
def _pad(x, pw):
    flat = tuple(v for pair in reversed(pw) for v in pair)
    return torch.nn.functional.pad(x, flat)

kernel_name = 'concat_heads__sliding_block__softmax_full_mha__prompt'

def setup_inputs():

    _w = (lambda a: (lambda t: t.to(torch.bfloat16) if t.is_floating_point() else t)(torch.as_tensor(a)))
    def _A_setup():
        B, S, D = 1, 2048, 512
        return {
            "x": _w((lambda *s: torch.randn(*s, dtype=torch.bfloat16) * 0.4)(B, S, D)),
            "wqkv": _w((lambda *s: torch.randn(*s, dtype=torch.bfloat16) * 0.4)(D, 3 * D)),
            "wo": _w((lambda *s: torch.randn(*s, dtype=torch.bfloat16) * 0.05)(D, D)),
        }
    _A_in = _A_setup()

    def _B_setup():
        B, S, D = 1, 2048, 512
        return {
            "x": _w((lambda *s: torch.randn(*s, dtype=torch.bfloat16) * 0.2)(B, S, D)),
            "wqkv": _w((lambda *s: torch.randn(*s, dtype=torch.bfloat16) * 0.4)(D, 3 * D)),
            "wo": _w((lambda *s: torch.randn(*s, dtype=torch.bfloat16) * 0.1)(D, D)),
        }
    _B_in = _B_setup()

    return {
        "A_x": _A_in["x"],
        "A_wqkv": _A_in["wqkv"],
        "A_wo": _A_in["wo"],
        "B_x": _B_in["x"],
        "B_wqkv": _B_in["wqkv"],
        "B_wo": _B_in["wo"],
    }

def reference(A_x, A_wqkv, A_wo, B_x, B_wqkv, B_wo):
    def _A_ref(x, wqkv, wo):
        B, S, D = x.shape
        H = 32; Dh = D // H; W = 256
        qkv = (lambda x, *d: x.permute(*d))((x @ wqkv).reshape(B, S, 3, H, Dh), 2, 0, 3, 1, 4)
        q, k, v = qkv[0], qkv[1], qkv[2]
        scores = torch.einsum("bhqd,bhkd->bhqk", q, k) * (Dh ** -0.5)
        idx = torch.arange(S)
        mask = (idx[:, None] - idx[None, :] >= 0) & (idx[:, None] - idx[None, :] < W)
        scores = torch.where(mask[None, None], scores, -1e9)
        p = _softmax(scores, axis=-1)
        o = torch.einsum("bhqk,bhkd->bhqd", p, v)
        return (lambda x, *d: x.permute(*d))(o, 0, 2, 1, 3).reshape(B, S, D) @ wo

    def _B_ref(x, wqkv, wo):
        B, S, D = x.shape
        H = 32; Dh = D // H
        qkv = (lambda x, *d: x.permute(*d))((x @ wqkv).reshape(B, S, 3, H, Dh), 2, 0, 3, 1, 4)
        q, k, v = qkv[0], qkv[1], qkv[2]
        scale = Dh ** -0.5
        scores = torch.einsum("bhqd,bhkd->bhqk", q, k) * scale
        p = _softmax(scores, axis=-1)
        o = torch.einsum("bhqk,bhkd->bhqd", p, v)
        return (lambda x, *d: x.permute(*d))(o, 0, 2, 1, 3).reshape(B, S, D) @ wo

    a = _A_ref(A_x, A_wqkv, A_wo)
    b = _B_ref(B_x, B_wqkv, B_wo)
    return torch.concatenate([a, b], axis=-1)
```

```cpp
#include <hip/hip_runtime.h>
#include <hip/hip_bf16.h>
#include <stdint.h>

typedef __attribute__((ext_vector_type(8))) short bf16x8;
typedef __attribute__((ext_vector_type(4))) short s16x4;
typedef __attribute__((ext_vector_type(16))) float f32x16;
typedef __attribute__((ext_vector_type(4))) unsigned u32x4;
typedef __attribute__((ext_vector_type(2))) unsigned u32x2;
typedef __attribute__((ext_vector_type(2))) float f32x2;
typedef __attribute__((ext_vector_type(2))) __bf16 bf16x2_t;
typedef unsigned short u16;
typedef __attribute__((address_space(3))) s16x4* lds_s16x4_p;

#define DI __device__ __forceinline__
#define MFMA32(a, b, c) __builtin_amdgcn_mfma_f32_32x32x16_bf16((a), (b), (c), 0, 0, 0)

constexpr int S_ = 2048, D_ = 512, H_ = 32, DH = 16;
constexpr float QSCALE = 0.25f * 1.4426950408889634f;
constexpr float THR = 12.f;

DI unsigned pk2(float a, float b) {
  f32x2 v = {a, b};
  bf16x2_t r = __builtin_convertvector(v, bf16x2_t);
  return __builtin_bit_cast(unsigned, r);
}
DI s16x4 tr_read(const char* p) { return __builtin_amdgcn_ds_read_tr16_b64_v4i16((lds_s16x4_p)p); }
DI bf16x8 cat44(s16x4 lo, s16x4 hi) { return __builtin_shufflevector(lo, hi, 0, 1, 2, 3, 4, 5, 6, 7); }

constexpr int G1_AS = 128 * 128;
constexpr int G1_BSTR = 448;
constexpr int G1_BS = 64 * G1_BSTR;
constexpr int G1_BUF = G1_AS + G1_BS;
constexpr int G1_LDS = 2 * G1_BUF;

__global__ __launch_bounds__(512) void qkv_gemm(const u16* __restrict__ xA, const u16* __restrict__ wA,
                                                const u16* __restrict__ xB, const u16* __restrict__ wB,
                                                u16* __restrict__ Qs, u16* __restrict__ Kp, u16* __restrict__ Vt) {
  extern __shared__ __attribute__((aligned(16))) char smem[];
  const int b = blockIdx.x, p = b >> 7, mt = (b & 127) >> 3, nt = b & 7;
  const u16* X = p ? xB : xA;
  const u16* W = p ? wB : wA;
  const int tid = threadIdx.x, w = __builtin_amdgcn_readfirstlane(tid >> 6), lane = tid & 63, r = lane & 31, h = lane >> 5;
  const int wm = w & 3, wn = w >> 2;
  int a_goff[2], a_loff[2], b_goff[3], b_loff[3];
#pragma unroll
  for (int i = 0; i < 2; ++i) {
    int c = tid + 512 * i, row = c >> 3, ch = c & 7;
    a_goff[i] = (mt * 128 + row) * 512 + ch * 8;
    a_loff[i] = row * 128 + ((ch ^ (row & 7)) << 4);
  }
#pragma unroll
  for (int i = 0; i < 3; ++i) {
    int c = tid + 512 * i, row = c / 24, ch = c % 24;
    b_goff[i] = row * 1536 + nt * 192 + ch * 8;
    b_loff[i] = G1_AS + row * G1_BSTR + ch * 16;
  }
  bf16x8 ra[2], rb[3];
#pragma unroll
  for (int i = 0; i < 2; ++i) ra[i] = *(const bf16x8*)(X + a_goff[i]);
#pragma unroll
  for (int i = 0; i < 3; ++i) rb[i] = *(const bf16x8*)(W + b_goff[i]);
#pragma unroll
  for (int i = 0; i < 2; ++i) *(bf16x8*)(smem + a_loff[i]) = ra[i];
#pragma unroll
  for (int i = 0; i < 3; ++i) *(bf16x8*)(smem + b_loff[i]) = rb[i];
  __syncthreads();

  f32x16 acc[3];
#pragma unroll
  for (int j = 0; j < 3; ++j)
#pragma unroll
    for (int i = 0; i < 16; ++i) acc[j][i] = 0.f;
  const int gcol0 = nt * 192 + wn * 96;
  const bool isV0 = gcol0 >= 1024, isV1 = gcol0 + 32 >= 1024, isV2 = gcol0 + 64 >= 1024;
  const int q4 = (lane & 15) >> 2, p4 = lane & 3, blk = (lane >> 4) & 1;
  const int a_rd = (wm * 32 + r) * 128;
  const int b_rd = G1_AS + (8 * h + q4) * G1_BSTR + (wn * 96 + 16 * blk + 4 * p4) * 2;

  for (int kt = 0; kt < 8; ++kt) {
    if (kt < 7) {
#pragma unroll
      for (int i = 0; i < 2; ++i) ra[i] = *(const bf16x8*)(X + a_goff[i] + (kt + 1) * 64);
#pragma unroll
      for (int i = 0; i < 3; ++i) rb[i] = *(const bf16x8*)(W + b_goff[i] + (kt + 1) * 64 * 1536);
    }
    const char* sb = smem + (kt & 1) * G1_BUF;
#pragma unroll
    for (int ks = 0; ks < 4; ++ks) {
      bf16x8 af = *(const bf16x8*)(sb + a_rd + (((2 * ks + h) ^ (r & 7)) << 4));
      bf16x8 bfr[3];
#pragma unroll
      for (int j = 0; j < 3; ++j) {
        const char* pb = sb + b_rd + ks * 16 * G1_BSTR + j * 64;
        bfr[j] = cat44(tr_read(pb), tr_read(pb + 4 * G1_BSTR));
      }
      if (isV0) acc[0] = MFMA32(af, bfr[0], acc[0]); else acc[0] = MFMA32(bfr[0], af, acc[0]);
      if (isV1) acc[1] = MFMA32(af, bfr[1], acc[1]); else acc[1] = MFMA32(bfr[1], af, acc[1]);
      if (isV2) acc[2] = MFMA32(af, bfr[2], acc[2]); else acc[2] = MFMA32(bfr[2], af, acc[2]);
    }
    if (kt < 7) {
      char* sn = smem + ((kt + 1) & 1) * G1_BUF;
#pragma unroll
      for (int i = 0; i < 2; ++i) *(bf16x8*)(sn + a_loff[i]) = ra[i];
#pragma unroll
      for (int i = 0; i < 3; ++i) *(bf16x8*)(sn + b_loff[i]) = rb[i];
    }
    __syncthreads();
  }
#pragma unroll
  for (int j = 0; j < 3; ++j) {
    const int gc = gcol0 + 32 * j;
    if (gc >= 1024) {
      const int n = gc - 1024 + r, hh = n >> 4, d = n & 15;
      u16* dst = Vt + ((size_t)(p * 32 + hh) * 16 + d) * 2048 + mt * 128 + wm * 32 + 4 * h;
#pragma unroll
      for (int g = 0; g < 4; ++g) {
        u32x2 v = {pk2(acc[j][4 * g], acc[j][4 * g + 1]), pk2(acc[j][4 * g + 2], acc[j][4 * g + 3])};
        *(u32x2*)(dst + 8 * g) = v;
      }
    } else {
      const bool isQ = gc < 512;
      const float sc = isQ ? QSCALE : 1.f;
      u16* base = isQ ? Qs : Kp;
      const int s = mt * 128 + wm * 32 + r;
#pragma unroll
      for (int g = 0; g < 4; ++g) {
        const int n = (gc & 511) + 8 * g + 4 * h, hh = n >> 4, d = n & 15;
        u32x2 v = {pk2(acc[j][4 * g] * sc, acc[j][4 * g + 1] * sc), pk2(acc[j][4 * g + 2] * sc, acc[j][4 * g + 3] * sc)};
        *(u32x2*)(base + ((size_t)(p * 32 + hh) * 2048 + s) * 16 + d) = v;
      }
    }
  }
}

constexpr int G3_AS = 64 * 128;
constexpr int G3_BSTR = 320;
constexpr int G3_BS = 64 * G3_BSTR;
constexpr int G3_BUF = G3_AS + G3_BS;
constexpr int G3_LDS = 2 * G3_BUF;

__global__ __launch_bounds__(512) void out_gemm(const u16* __restrict__ Oc, const u16* __restrict__ woA,
                                                const u16* __restrict__ woB, u16* __restrict__ out) {
  extern __shared__ __attribute__((aligned(16))) char smem[];
  const int b = blockIdx.x, p = b >> 7, mt = (b & 127) >> 2, nt = b & 3;
  const u16* X = Oc + (size_t)p * 2048 * 512;
  const u16* W = p ? woB : woA;
  const int tid = threadIdx.x, w = __builtin_amdgcn_readfirstlane(tid >> 6), lane = tid & 63, r = lane & 31, h = lane >> 5;
  const int wm = w & 1, wn = w >> 1;
  int a_goff, a_loff, b_goff[2], b_loff[2];
  {
    int row = tid >> 3, ch = tid & 7;
    a_goff = (mt * 64 + row) * 512 + ch * 8;
    a_loff = row * 128 + ((ch ^ (row & 7)) << 4);
  }
#pragma unroll
  for (int i = 0; i < 2; ++i) {
    int c = tid + 512 * i, row = c >> 4, ch = c & 15;
    b_goff[i] = row * 512 + nt * 128 + ch * 8;
    b_loff[i] = G3_AS + row * G3_BSTR + ch * 16;
  }
  bf16x8 ra, rb[2];
  ra = *(const bf16x8*)(X + a_goff);
#pragma unroll
  for (int i = 0; i < 2; ++i) rb[i] = *(const bf16x8*)(W + b_goff[i]);
  *(bf16x8*)(smem + a_loff) = ra;
#pragma unroll
  for (int i = 0; i < 2; ++i) *(bf16x8*)(smem + b_loff[i]) = rb[i];
  __syncthreads();
  f32x16 acc;
#pragma unroll
  for (int i = 0; i < 16; ++i) acc[i] = 0.f;
  const int q4 = (lane & 15) >> 2, p4 = lane & 3, blk = (lane >> 4) & 1;
  const int a_rd = (wm * 32 + r) * 128;
  const int b_rd = G3_AS + (8 * h + q4) * G3_BSTR + (wn * 32 + 16 * blk + 4 * p4) * 2;
  for (int kt = 0; kt < 8; ++kt) {
    if (kt < 7) {
      ra = *(const bf16x8*)(X + a_goff + (kt + 1) * 64);
#pragma unroll
      for (int i = 0; i < 2; ++i) rb[i] = *(const bf16x8*)(W + b_goff[i] + (kt + 1) * 64 * 512);
    }
    const char* sb = smem + (kt & 1) * G3_BUF;
#pragma unroll
    for (int ks = 0; ks < 4; ++ks) {
      bf16x8 af = *(const bf16x8*)(sb + a_rd + (((2 * ks + h) ^ (r & 7)) << 4));
      const char* pb = sb + b_rd + ks * 16 * G3_BSTR;
      bf16x8 bfr = cat44(tr_read(pb), tr_read(pb + 4 * G3_BSTR));
      acc = MFMA32(bfr, af, acc);
    }
    if (kt < 7) {
      char* sn = smem + ((kt + 1) & 1) * G3_BUF;
      *(bf16x8*)(sn + a_loff) = ra;
#pragma unroll
      for (int i = 0; i < 2; ++i) *(bf16x8*)(sn + b_loff[i]) = rb[i];
    }
    __syncthreads();
  }
  const int s = mt * 64 + wm * 32 + r;
  u16* dst = out + (size_t)s * 1024 + p * 512 + nt * 128 + wn * 32 + 4 * h;
#pragma unroll
  for (int g = 0; g < 4; ++g) {
    u32x2 v = {pk2(acc[4 * g], acc[4 * g + 1]), pk2(acc[4 * g + 2], acc[4 * g + 3])};
    *(u32x2*)(dst + 8 * g) = v;
  }
}

constexpr int AT_VSTR = 4112;
constexpr int AT_K = 0, AT_V = 65536;
constexpr int AT_LDS = 65536 + 16 * AT_VSTR;

DI float max16(const f32x16& s) {
  float a = fmaxf(fmaxf(s[0], s[1]), s[2]);
  float b = fmaxf(fmaxf(s[3], s[4]), s[5]);
  float c = fmaxf(fmaxf(s[6], s[7]), s[8]);
  float d = fmaxf(fmaxf(s[9], s[10]), s[11]);
  float e = fmaxf(fmaxf(s[12], s[13]), s[14]);
  a = fmaxf(fmaxf(a, b), c);
  d = fmaxf(fmaxf(d, e), s[15]);
  return fmaxf(a, d);
}

DI void attn_tile(f32x16 S, const bf16x8& vf0, const bf16x8& vf1, f32x16& negm, f32x16& O, bool first) {
  float mx = max16(S);
  if (first || __any(mx > THR)) {
    float mo = __shfl_xor(mx, 32);
    float delta = fmaxf(mx, mo);
    if (!first) delta = fmaxf(delta, 0.f);
#pragma unroll
    for (int i = 0; i < 16; ++i) { S[i] -= delta; negm[i] -= delta; }
    if (!first) {
      float alpha = __builtin_amdgcn_exp2f(-delta);
#pragma unroll
      for (int i = 0; i < 16; ++i) O[i] *= alpha;
    }
  }
#pragma unroll
  for (int i = 0; i < 16; ++i) S[i] = __builtin_amdgcn_exp2f(S[i]);
  u32x4 p0 = {pk2(S[0], S[1]), pk2(S[2], S[3]), pk2(S[4], S[5]), pk2(S[6], S[7])};
  u32x4 p1 = {pk2(S[8], S[9]), pk2(S[10], S[11]), pk2(S[12], S[13]), pk2(S[14], S[15])};
  O = MFMA32(vf0, __builtin_bit_cast(bf16x8, p0), O);
  O = MFMA32(vf1, __builtin_bit_cast(bf16x8, p1), O);
}

DI void attn_finish(const f32x16& O, u16* dst  , int r, int h) {
  float l = __shfl(O[8], r);
  float inv = 1.f / l;
  u32x2 v0 = {pk2(O[0] * inv, O[1] * inv), pk2(O[2] * inv, O[3] * inv)};
  u32x2 v1 = {pk2(O[4] * inv, O[5] * inv), pk2(O[6] * inv, O[7] * inv)};
  *(u32x2*)(dst + 4 * h) = v0;
  *(u32x2*)(dst + 8 + 4 * h) = v1;
}

__global__ __launch_bounds__(512) void attn_kernel(const u16* __restrict__ Qs, const u16* __restrict__ Kp,
                                                   const u16* __restrict__ Vt, u16* __restrict__ Oc) {
  extern __shared__ __attribute__((aligned(16))) char smem[];
  const int b = blockIdx.x, xg = b & 7, idx = b >> 3;
  const int head = xg * 4 + (idx & 3), chunk = idx >> 2;
  const int tid = threadIdx.x, w = __builtin_amdgcn_readfirstlane(tid >> 6), lane = tid & 63, r = lane & 31, h = lane >> 5;
  const int q0 = chunk * 256 + w * 32;
  const int pr = (r & ~0xC) | ((r & 4) << 1) | ((r & 8) >> 1);

  {
    const u16* Kg = Kp + (size_t)(32 + head) * 2048 * 16;
    const u16* Vg = Vt + (size_t)(32 + head) * 16 * 2048;
#pragma unroll
    for (int i = 0; i < 8; ++i) {
      const int pc = w * 8 + i;
      __builtin_amdgcn_global_load_lds((const void*)(Kg + pc * 512 + lane * 8),
                                       (__attribute__((address_space(3))) void*)(smem + AT_K + pc * 1024), 16, 0, 0);
    }
#pragma unroll
    for (int i = 0; i < 8; ++i) {
      const int pc = w * 8 + i, d = pc >> 2, part = pc & 3;
      __builtin_amdgcn_global_load_lds((const void*)(Vg + d * 2048 + part * 512 + lane * 8),
                                       (__attribute__((address_space(3))) void*)(smem + AT_V + d * AT_VSTR + part * 1024), 16, 0, 0);
    }
    if (tid == 0) {
      u32x4 ones = {0x3f803f80u, 0x3f803f80u, 0x3f803f80u, 0x3f803f80u};
      u32x4 zeros = {0u, 0u, 0u, 0u};
      *(u32x4*)(smem + AT_V + 4096) = ones;
      *(u32x4*)(smem + AT_V + AT_VSTR + 4096) = zeros;
    }
  }

  f32x16 negm, O;
  {
    const u16* Kg = Kp + (size_t)head * 2048 * 16;
    const u16* Vg = Vt + (size_t)head * 16 * 2048;
    const bf16x8 qf = *(const bf16x8*)(Qs + ((size_t)head * 2048 + q0 + r) * 16 + 8 * h);
#pragma unroll
    for (int i = 0; i < 16; ++i) { negm[i] = 0.f; O[i] = 0.f; }
    const short cval = (r == 16) ? (short)0x3f80 : (short)0;
    const bf16x8 vconst = {cval, cval, cval, cval, cval, cval, cval, cval};
    const int dd = r & 15;
    const int tmin = q0 >= 256 ? 0 : (256 - q0) >> 5;
    for (int t = 8; t >= tmin; --t) {
      const int key0 = q0 - 256 + 32 * t;
      bf16x8 kf = *(const bf16x8*)(Kg + (size_t)(key0 + pr) * 16 + 8 * h);
      bf16x8 v0 = *(const bf16x8*)(Vg + dd * 2048 + key0 + 8 * h);
      bf16x8 v1 = *(const bf16x8*)(Vg + dd * 2048 + key0 + 16 + 8 * h);
      if (r >= 16) { v0 = vconst; v1 = vconst; }
      f32x16 S = MFMA32(kf, qf, negm);
      if (t == 8 || t == 0) {
#pragma unroll
        for (int i = 0; i < 16; ++i) {
          const int ko = (i & 3) + 4 * ((i >> 2) & 1) + 8 * h + 16 * (i >> 3);
          const bool valid = (t == 8) ? (ko <= r) : (ko > r);
          S[i] = valid ? S[i] : -INFINITY;
        }
      }
      attn_tile(S, v0, v1, negm, O, t == 8);
    }
    attn_finish(O, Oc + (size_t)(q0 + r) * 512 + head * 16, r, h);
  }

  __builtin_amdgcn_s_waitcnt(0x0f70);
  __syncthreads();
  {
    const bf16x8 qf = *(const bf16x8*)(Qs + ((size_t)(32 + head) * 2048 + q0 + r) * 16 + 8 * h);
#pragma unroll
    for (int i = 0; i < 16; ++i) { negm[i] = 0.f; O[i] = 0.f; }
    const char* kp = smem + AT_K + pr * 32 + 16 * h;
    int va0, va1, vinc;
    if (r < 16) { va0 = AT_V + r * AT_VSTR + 16 * h; va1 = va0 + 32; vinc = 64; }
    else { va0 = AT_V + (r == 16 ? 4096 : AT_VSTR + 4096); va1 = va0; vinc = 0; }
#pragma unroll 4
    for (int t = 0; t < 64; ++t) {
      bf16x8 kf = *(const bf16x8*)(kp + t * 1024);
      bf16x8 v0 = *(const bf16x8*)(smem + va0);
      bf16x8 v1 = *(const bf16x8*)(smem + va1);
      va0 += vinc; va1 += vinc;
      f32x16 S = MFMA32(kf, qf, negm);
      attn_tile(S, v0, v1, negm, O, t == 0);
    }
    attn_finish(O, Oc + (size_t)(2048 + q0 + r) * 512 + head * 16, r, h);
  }
}

extern "C" void kernel_launch(void* const* d_in, const int* in_sizes, int n_in, void* d_out, int out_size, void* d_ws,
                              size_t ws_size, hipStream_t stream) {
  const u16* Ax = (const u16*)d_in[0];
  const u16* Awqkv = (const u16*)d_in[1];
  const u16* Awo = (const u16*)d_in[2];
  const u16* Bx = (const u16*)d_in[3];
  const u16* Bwqkv = (const u16*)d_in[4];
  const u16* Bwo = (const u16*)d_in[5];
  char* ws = (char*)d_ws;
  u16* Qs = (u16*)(ws);
  u16* Kp = (u16*)(ws + (4 << 20));
  u16* Vt = (u16*)(ws + (8 << 20));
  u16* Oc = (u16*)(ws + (12 << 20));
  (void)hipFuncSetAttribute((const void*)qkv_gemm, hipFuncAttributeMaxDynamicSharedMemorySize, G1_LDS);
  (void)hipFuncSetAttribute((const void*)attn_kernel, hipFuncAttributeMaxDynamicSharedMemorySize, AT_LDS);
  (void)hipFuncSetAttribute((const void*)out_gemm, hipFuncAttributeMaxDynamicSharedMemorySize, G3_LDS);
  qkv_gemm<<<256, 512, G1_LDS, stream>>>(Ax, Awqkv, Bx, Bwqkv, Qs, Kp, Vt);
  attn_kernel<<<256, 512, AT_LDS, stream>>>(Qs, Kp, Vt, Oc);
  out_gemm<<<256, 512, G3_LDS, stream>>>(Oc, Awo, Bwo, (u16*)d_out);
}
```

```cpp
#include <hip/hip_runtime.h>
#include <hip/hip_bf16.h>
#include <stdint.h>

typedef __attribute__((ext_vector_type(8))) short bf16x8;
typedef __attribute__((ext_vector_type(4))) short s16x4;
typedef __attribute__((ext_vector_type(16))) float f32x16;
typedef __attribute__((ext_vector_type(4))) unsigned u32x4;
typedef __attribute__((ext_vector_type(2))) unsigned u32x2;
typedef __attribute__((ext_vector_type(2))) float f32x2;
typedef __attribute__((ext_vector_type(2))) __bf16 bf16x2_t;
typedef unsigned short u16;
typedef __attribute__((address_space(3))) s16x4* lds_s16x4_p;

#define DI __device__ __forceinline__
#define MFMA32(a, b, c) __builtin_amdgcn_mfma_f32_32x32x16_bf16((a), (b), (c), 0, 0, 0)

constexpr int S_ = 2048, D_ = 512, H_ = 32, DH = 16;
constexpr float QSCALE = 0.25f * 1.4426950408889634f;
constexpr float THR = 12.f;

DI unsigned pk2(float a, float b) {
  f32x2 v = {a, b};
  bf16x2_t r = __builtin_convertvector(v, bf16x2_t);
  return __builtin_bit_cast(unsigned, r);
}
DI s16x4 tr_read(const char* p) { return __builtin_amdgcn_ds_read_tr16_b64_v4i16((lds_s16x4_p)p); }
DI bf16x8 cat44(s16x4 lo, s16x4 hi) { return __builtin_shufflevector(lo, hi, 0, 1, 2, 3, 4, 5, 6, 7); }

constexpr int G1_AS = 128 * 128;
constexpr int G1_BSTR = 448;
constexpr int G1_BS = 64 * G1_BSTR;
constexpr int G1_BUF = G1_AS + G1_BS;
constexpr int G1_LDS = 2 * G1_BUF;

__global__ __launch_bounds__(512) void qkv_gemm(const u16* __restrict__ xA, const u16* __restrict__ wA,
                                                const u16* __restrict__ xB, const u16* __restrict__ wB,
                                                u16* __restrict__ Qs, u16* __restrict__ Kp, u16* __restrict__ Vt) {
  extern __shared__ __attribute__((aligned(16))) char smem[];
  const int b = blockIdx.x, xg = b & 7, idx = b >> 3, p = xg & 1, mt = (xg >> 1) * 4 + (idx >> 3), nt = idx & 7;
  const u16* X = p ? xB : xA;
  const u16* W = p ? wB : wA;
  const int tid = threadIdx.x, w = __builtin_amdgcn_readfirstlane(tid >> 6), lane = tid & 63, r = lane & 31, h = lane >> 5;
  const int wm = w & 3, wn = w >> 2;
  int a_goff[2], a_loff[2], b_goff[3], b_loff[3];
#pragma unroll
  for (int i = 0; i < 2; ++i) {
    int c = tid + 512 * i, row = c >> 3, ch = c & 7;
    a_goff[i] = (mt * 128 + row) * 512 + ch * 8;
    a_loff[i] = row * 128 + ((ch ^ (row & 7)) << 4);
  }
#pragma unroll
  for (int i = 0; i < 3; ++i) {
    int c = tid + 512 * i, row = c / 24, ch = c % 24;
    b_goff[i] = row * 1536 + nt * 192 + ch * 8;
    b_loff[i] = G1_AS + row * G1_BSTR + ch * 16;
  }
  bf16x8 ra[2], rb[3];
#pragma unroll
  for (int i = 0; i < 2; ++i) ra[i] = *(const bf16x8*)(X + a_goff[i]);
#pragma unroll
  for (int i = 0; i < 3; ++i) rb[i] = *(const bf16x8*)(W + b_goff[i]);
#pragma unroll
  for (int i = 0; i < 2; ++i) *(bf16x8*)(smem + a_loff[i]) = ra[i];
#pragma unroll
  for (int i = 0; i < 3; ++i) *(bf16x8*)(smem + b_loff[i]) = rb[i];
  __syncthreads();

  f32x16 acc[3];
#pragma unroll
  for (int j = 0; j < 3; ++j)
#pragma unroll
    for (int i = 0; i < 16; ++i) acc[j][i] = 0.f;
  const int gcol0 = nt * 192 + wn * 96;
  const bool isV0 = gcol0 >= 1024, isV1 = gcol0 + 32 >= 1024, isV2 = gcol0 + 64 >= 1024;
  const int q4 = (lane & 15) >> 2, p4 = lane & 3, blk = (lane >> 4) & 1;
  const int a_rd = (wm * 32 + r) * 128;
  const int b_rd = G1_AS + (8 * h + q4) * G1_BSTR + (wn * 96 + 16 * blk + 4 * p4) * 2;

  for (int kt = 0; kt < 8; ++kt) {
    if (kt < 7) {
#pragma unroll
      for (int i = 0; i < 2; ++i) ra[i] = *(const bf16x8*)(X + a_goff[i] + (kt + 1) * 64);
#pragma unroll
      for (int i = 0; i < 3; ++i) rb[i] = *(const bf16x8*)(W + b_goff[i] + (kt + 1) * 64 * 1536);
    }
    const char* sb = smem + (kt & 1) * G1_BUF;
#pragma unroll
    for (int ks = 0; ks < 4; ++ks) {
      bf16x8 af = *(const bf16x8*)(sb + a_rd + (((2 * ks + h) ^ (r & 7)) << 4));
      bf16x8 bfr[3];
#pragma unroll
      for (int j = 0; j < 3; ++j) {
        const char* pb = sb + b_rd + ks * 16 * G1_BSTR + j * 64;
        bfr[j] = cat44(tr_read(pb), tr_read(pb + 4 * G1_BSTR));
      }
      if (isV0) acc[0] = MFMA32(af, bfr[0], acc[0]); else acc[0] = MFMA32(bfr[0], af, acc[0]);
      if (isV1) acc[1] = MFMA32(af, bfr[1], acc[1]); else acc[1] = MFMA32(bfr[1], af, acc[1]);
      if (isV2) acc[2] = MFMA32(af, bfr[2], acc[2]); else acc[2] = MFMA32(bfr[2], af, acc[2]);
    }
    if (kt < 7) {
      char* sn = smem + ((kt + 1) & 1) * G1_BUF;
#pragma unroll
      for (int i = 0; i < 2; ++i) *(bf16x8*)(sn + a_loff[i]) = ra[i];
#pragma unroll
      for (int i = 0; i < 3; ++i) *(bf16x8*)(sn + b_loff[i]) = rb[i];
    }
    __syncthreads();
  }
#pragma unroll
  for (int j = 0; j < 3; ++j) {
    const int gc = gcol0 + 32 * j;
    if (gc >= 1024) {
      const int n = gc - 1024 + r, hh = n >> 4, d = n & 15;
      u16* dst = Vt + ((size_t)(p * 32 + hh) * 16 + d) * 2048 + mt * 128 + wm * 32 + 4 * h;
#pragma unroll
      for (int g = 0; g < 4; ++g) {
        u32x2 v = {pk2(acc[j][4 * g], acc[j][4 * g + 1]), pk2(acc[j][4 * g + 2], acc[j][4 * g + 3])};
        *(u32x2*)(dst + 8 * g) = v;
      }
    } else {
      const bool isQ = gc < 512;
      const float sc = isQ ? QSCALE : 1.f;
      u16* base = isQ ? Qs : Kp;
      const int s = mt * 128 + wm * 32 + r;
#pragma unroll
      for (int g = 0; g < 4; ++g) {
        const int n = (gc & 511) + 8 * g + 4 * h, hh = n >> 4, d = n & 15;
        u32x2 v = {pk2(acc[j][4 * g] * sc, acc[j][4 * g + 1] * sc), pk2(acc[j][4 * g + 2] * sc, acc[j][4 * g + 3] * sc)};
        *(u32x2*)(base + ((size_t)(p * 32 + hh) * 2048 + s) * 16 + d) = v;
      }
    }
  }
}

constexpr int G3_AS = 64 * 128;
constexpr int G3_BSTR = 320;
constexpr int G3_BS = 64 * G3_BSTR;
constexpr int G3_BUF = G3_AS + G3_BS;
constexpr int G3_LDS = 2 * G3_BUF;

__global__ __launch_bounds__(512) void out_gemm(const u16* __restrict__ Oc, const u16* __restrict__ woA,
                                                const u16* __restrict__ woB, u16* __restrict__ out) {
  extern __shared__ __attribute__((aligned(16))) char smem[];
  const int b = blockIdx.x, xg = b & 7, idx = b >> 3, p = xg & 1, mt = (xg >> 1) * 8 + (idx >> 2), nt = idx & 3;
  const u16* X = Oc + (size_t)p * 2048 * 512;
  const u16* W = p ? woB : woA;
  const int tid = threadIdx.x, w = __builtin_amdgcn_readfirstlane(tid >> 6), lane = tid & 63, r = lane & 31, h = lane >> 5;
  const int wm = w & 1, wn = w >> 1;
  int a_goff, a_loff, b_goff[2], b_loff[2];
  {
    int row = tid >> 3, ch = tid & 7;
    a_goff = (mt * 64 + row) * 512 + ch * 8;
    a_loff = row * 128 + ((ch ^ (row & 7)) << 4);
  }
#pragma unroll
  for (int i = 0; i < 2; ++i) {
    int c = tid + 512 * i, row = c >> 4, ch = c & 15;
    b_goff[i] = row * 512 + nt * 128 + ch * 8;
    b_loff[i] = G3_AS + row * G3_BSTR + ch * 16;
  }
  bf16x8 ra, rb[2];
  ra = *(const bf16x8*)(X + a_goff);
#pragma unroll
  for (int i = 0; i < 2; ++i) rb[i] = *(const bf16x8*)(W + b_goff[i]);
  *(bf16x8*)(smem + a_loff) = ra;
#pragma unroll
  for (int i = 0; i < 2; ++i) *(bf16x8*)(smem + b_loff[i]) = rb[i];
  __syncthreads();
  f32x16 acc;
#pragma unroll
  for (int i = 0; i < 16; ++i) acc[i] = 0.f;
  const int q4 = (lane & 15) >> 2, p4 = lane & 3, blk = (lane >> 4) & 1;
  const int a_rd = (wm * 32 + r) * 128;
  const int b_rd = G3_AS + (8 * h + q4) * G3_BSTR + (wn * 32 + 16 * blk + 4 * p4) * 2;
  for (int kt = 0; kt < 8; ++kt) {
    if (kt < 7) {
      ra = *(const bf16x8*)(X + a_goff + (kt + 1) * 64);
#pragma unroll
      for (int i = 0; i < 2; ++i) rb[i] = *(const bf16x8*)(W + b_goff[i] + (kt + 1) * 64 * 512);
    }
    const char* sb = smem + (kt & 1) * G3_BUF;
#pragma unroll
    for (int ks = 0; ks < 4; ++ks) {
      bf16x8 af = *(const bf16x8*)(sb + a_rd + (((2 * ks + h) ^ (r & 7)) << 4));
      const char* pb = sb + b_rd + ks * 16 * G3_BSTR;
      bf16x8 bfr = cat44(tr_read(pb), tr_read(pb + 4 * G3_BSTR));
      acc = MFMA32(bfr, af, acc);
    }
    if (kt < 7) {
      char* sn = smem + ((kt + 1) & 1) * G3_BUF;
      *(bf16x8*)(sn + a_loff) = ra;
#pragma unroll
      for (int i = 0; i < 2; ++i) *(bf16x8*)(sn + b_loff[i]) = rb[i];
    }
    __syncthreads();
  }
  const int s = mt * 64 + wm * 32 + r;
  u16* dst = out + (size_t)s * 1024 + p * 512 + nt * 128 + wn * 32 + 4 * h;
#pragma unroll
  for (int g = 0; g < 4; ++g) {
    u32x2 v = {pk2(acc[4 * g], acc[4 * g + 1]), pk2(acc[4 * g + 2], acc[4 * g + 3])};
    *(u32x2*)(dst + 8 * g) = v;
  }
}

constexpr int AT_VSTR = 4112;
constexpr int AT_K = 0, AT_V = 65536;
constexpr int AT_LDS = 65536 + 16 * AT_VSTR;

DI float max16(const f32x16& s) {
  float a = fmaxf(fmaxf(s[0], s[1]), s[2]);
  float b = fmaxf(fmaxf(s[3], s[4]), s[5]);
  float c = fmaxf(fmaxf(s[6], s[7]), s[8]);
  float d = fmaxf(fmaxf(s[9], s[10]), s[11]);
  float e = fmaxf(fmaxf(s[12], s[13]), s[14]);
  a = fmaxf(fmaxf(a, b), c);
  d = fmaxf(fmaxf(d, e), s[15]);
  return fmaxf(a, d);
}

DI void attn_tile(f32x16 S, const bf16x8& vf0, const bf16x8& vf1, f32x16& negm, f32x16& O, bool first, f32x16& Sn) {
  float mx = max16(S);
  if (first || __any(mx > THR)) {
    float mo = __shfl_xor(mx, 32);
    float delta = fmaxf(mx, mo);
    if (!first) delta = fmaxf(delta, 0.f);
#pragma unroll
    for (int i = 0; i < 16; ++i) { S[i] -= delta; negm[i] -= delta; Sn[i] -= delta; }
    if (!first) {
      float alpha = __builtin_amdgcn_exp2f(-delta);
#pragma unroll
      for (int i = 0; i < 16; ++i) O[i] *= alpha;
    }
  }
#pragma unroll
  for (int i = 0; i < 16; ++i) S[i] = __builtin_amdgcn_exp2f(S[i]);
  u32x4 p0 = {pk2(S[0], S[1]), pk2(S[2], S[3]), pk2(S[4], S[5]), pk2(S[6], S[7])};
  u32x4 p1 = {pk2(S[8], S[9]), pk2(S[10], S[11]), pk2(S[12], S[13]), pk2(S[14], S[15])};
  O = MFMA32(vf0, __builtin_bit_cast(bf16x8, p0), O);
  O = MFMA32(vf1, __builtin_bit_cast(bf16x8, p1), O);
}

DI void attn_finish(const f32x16& O, u16* dst  , int r, int h) {
  float l = __shfl(O[8], r);
  float inv = 1.f / l;
  u32x2 v0 = {pk2(O[0] * inv, O[1] * inv), pk2(O[2] * inv, O[3] * inv)};
  u32x2 v1 = {pk2(O[4] * inv, O[5] * inv), pk2(O[6] * inv, O[7] * inv)};
  *(u32x2*)(dst + 4 * h) = v0;
  *(u32x2*)(dst + 8 + 4 * h) = v1;
}

__global__ __launch_bounds__(512) void attn_kernel(const u16* __restrict__ Qs, const u16* __restrict__ Kp,
                                                   const u16* __restrict__ Vt, u16* __restrict__ Oc) {
  extern __shared__ __attribute__((aligned(16))) char smem[];
  const int b = blockIdx.x, xg = b & 7, idx = b >> 3;
  const int head = xg * 4 + (idx & 3), chunk = idx >> 2;
  const int tid = threadIdx.x, w = __builtin_amdgcn_readfirstlane(tid >> 6), lane = tid & 63, r = lane & 31, h = lane >> 5;
  const int q0 = chunk * 256 + w * 32;
  const int pr = (r & ~0xC) | ((r & 4) << 1) | ((r & 8) >> 1);

  {
    const u16* Kg = Kp + (size_t)(32 + head) * 2048 * 16;
    const u16* Vg = Vt + (size_t)(32 + head) * 16 * 2048;
#pragma unroll
    for (int i = 0; i < 8; ++i) {
      const int pc = w * 8 + i;
      __builtin_amdgcn_global_load_lds((const void*)(Kg + pc * 512 + lane * 8),
                                       (__attribute__((address_space(3))) void*)(smem + AT_K + pc * 1024), 16, 0, 0);
    }
#pragma unroll
    for (int i = 0; i < 8; ++i) {
      const int pc = w * 8 + i, d = pc >> 2, part = pc & 3;
      __builtin_amdgcn_global_load_lds((const void*)(Vg + d * 2048 + part * 512 + lane * 8),
                                       (__attribute__((address_space(3))) void*)(smem + AT_V + d * AT_VSTR + part * 1024), 16, 0, 0);
    }
    if (tid == 0) {
      u32x4 ones = {0x3f803f80u, 0x3f803f80u, 0x3f803f80u, 0x3f803f80u};
      u32x4 zeros = {0u, 0u, 0u, 0u};
      *(u32x4*)(smem + AT_V + 4096) = ones;
      *(u32x4*)(smem + AT_V + AT_VSTR + 4096) = zeros;
    }
  }

  f32x16 negm, O;
  {
    const u16* Kg = Kp + (size_t)head * 2048 * 16;
    const u16* Vg = Vt + (size_t)head * 16 * 2048;
    const bf16x8 qf = *(const bf16x8*)(Qs + ((size_t)head * 2048 + q0 + r) * 16 + 8 * h);
#pragma unroll
    for (int i = 0; i < 16; ++i) { negm[i] = 0.f; O[i] = 0.f; }
    const short cval = (r == 16) ? (short)0x3f80 : (short)0;
    const bf16x8 vconst = {cval, cval, cval, cval, cval, cval, cval, cval};
    const int dd = r & 15;
    const int tmin = q0 >= 256 ? 0 : (256 - q0) >> 5;
    bf16x8 kf[9], v0[9], v1[9];
#pragma unroll
    for (int t = 0; t < 9; ++t) {
      const int key0 = max(q0 - 256 + 32 * t, 0);
      kf[t] = *(const bf16x8*)(Kg + (size_t)(key0 + pr) * 16 + 8 * h);
      v0[t] = *(const bf16x8*)(Vg + dd * 2048 + key0 + 8 * h);
      v1[t] = *(const bf16x8*)(Vg + dd * 2048 + key0 + 16 + 8 * h);
    }
    f32x16 dummy;
#pragma unroll
    for (int t = 8; t >= 0; --t) {
      if (t >= tmin) {
        if (r >= 16) { v0[t] = vconst; v1[t] = vconst; }
        f32x16 S = MFMA32(kf[t], qf, negm);
        if (t == 8 || t == 0) {
#pragma unroll
          for (int i = 0; i < 16; ++i) {
            const int ko = (i & 3) + 4 * ((i >> 2) & 1) + 8 * h + 16 * (i >> 3);
            const bool valid = (t == 8) ? (ko <= r) : (ko > r);
            S[i] = valid ? S[i] : -INFINITY;
          }
        }
        attn_tile(S, v0[t], v1[t], negm, O, t == 8, dummy);
      }
    }
    attn_finish(O, Oc + (size_t)(q0 + r) * 512 + head * 16, r, h);
  }

  __builtin_amdgcn_s_waitcnt(0x0f70);
  __syncthreads();
  {
    const bf16x8 qf = *(const bf16x8*)(Qs + ((size_t)(32 + head) * 2048 + q0 + r) * 16 + 8 * h);
#pragma unroll
    for (int i = 0; i < 16; ++i) { negm[i] = 0.f; O[i] = 0.f; }
    const char* kp = smem + AT_K + pr * 32 + 16 * h;
    int va0, va1, vinc;
    if (r < 16) { va0 = AT_V + r * AT_VSTR + 16 * h; va1 = va0 + 32; vinc = 64; }
    else { va0 = AT_V + (r == 16 ? 4096 : AT_VSTR + 4096); va1 = va0; vinc = 0; }
    f32x16 Sn = MFMA32(*(const bf16x8*)(kp), qf, negm);
    bf16x8 kfn = *(const bf16x8*)(kp + 1024);
#pragma unroll 4
#define NBT 64
    for (int t = 0; t < NBT; ++t) {
      bf16x8 v0 = *(const bf16x8*)(smem + va0);
      bf16x8 v1 = *(const bf16x8*)(smem + va1);
      va0 += vinc; va1 += vinc;
      f32x16 S = Sn;
      Sn = MFMA32(kfn, qf, negm);
      kfn = *(const bf16x8*)(kp + ((t + 2) & 63) * 1024);
      attn_tile(S, v0, v1, negm, O, t == 0, Sn);
    }
    attn_finish(O, Oc + (size_t)(2048 + q0 + r) * 512 + head * 16, r, h);
  }
}

extern "C" void kernel_launch(void* const* d_in, const int* in_sizes, int n_in, void* d_out, int out_size, void* d_ws,
                              size_t ws_size, hipStream_t stream) {
  const u16* Ax = (const u16*)d_in[0];
  const u16* Awqkv = (const u16*)d_in[1];
  const u16* Awo = (const u16*)d_in[2];
  const u16* Bx = (const u16*)d_in[3];
  const u16* Bwqkv = (const u16*)d_in[4];
  const u16* Bwo = (const u16*)d_in[5];
  char* ws = (char*)d_ws;
  u16* Qs = (u16*)(ws);
  u16* Kp = (u16*)(ws + (4 << 20));
  u16* Vt = (u16*)(ws + (8 << 20));
  u16* Oc = (u16*)(ws + (12 << 20));
  (void)hipFuncSetAttribute((const void*)qkv_gemm, hipFuncAttributeMaxDynamicSharedMemorySize, G1_LDS);
  (void)hipFuncSetAttribute((const void*)attn_kernel, hipFuncAttributeMaxDynamicSharedMemorySize, AT_LDS);
  (void)hipFuncSetAttribute((const void*)out_gemm, hipFuncAttributeMaxDynamicSharedMemorySize, G3_LDS);
  qkv_gemm<<<256, 512, G1_LDS, stream>>>(Ax, Awqkv, Bx, Bwqkv, Qs, Kp, Vt);
  attn_kernel<<<256, 512, AT_LDS, stream>>>(Qs, Kp, Vt, Oc);
  out_gemm<<<256, 512, G3_LDS, stream>>>(Oc, Awo, Bwo, (u16*)d_out);
}
```

```cpp
#include <hip/hip_runtime.h>
#include <hip/hip_bf16.h>
#include <stdint.h>

typedef __attribute__((ext_vector_type(8))) short bf16x8;
typedef __attribute__((ext_vector_type(4))) short s16x4;
typedef __attribute__((ext_vector_type(16))) float f32x16;
typedef __attribute__((ext_vector_type(4))) unsigned u32x4;
typedef __attribute__((ext_vector_type(2))) unsigned u32x2;
typedef __attribute__((ext_vector_type(2))) float f32x2;
typedef __attribute__((ext_vector_type(2))) __bf16 bf16x2_t;
typedef unsigned short u16;
typedef __attribute__((address_space(3))) s16x4* lds_s16x4_p;

#define DI __device__ __forceinline__
#define MFMA32(a, b, c) __builtin_amdgcn_mfma_f32_32x32x16_bf16((a), (b), (c), 0, 0, 0)

constexpr int S_ = 2048, D_ = 512, H_ = 32, DH = 16;
constexpr float QSCALE = 0.25f * 1.4426950408889634f;
constexpr float THR = 12.f;

DI unsigned pk2(float a, float b) {
  f32x2 v = {a, b};
  bf16x2_t r = __builtin_convertvector(v, bf16x2_t);
  return __builtin_bit_cast(unsigned, r);
}
DI s16x4 tr_read(const char* p) { return __builtin_amdgcn_ds_read_tr16_b64_v4i16((lds_s16x4_p)p); }
DI bf16x8 cat44(s16x4 lo, s16x4 hi) { return __builtin_shufflevector(lo, hi, 0, 1, 2, 3, 4, 5, 6, 7); }

constexpr int G1_AS = 128 * 128;
constexpr int G1_BSTR = 448;
constexpr int G1_BS = 64 * G1_BSTR;
constexpr int G1_BUF = G1_AS + G1_BS;
constexpr int G1_LDS = 2 * G1_BUF;

__global__ __launch_bounds__(512) void qkv_gemm(const u16* __restrict__ xA, const u16* __restrict__ wA,
                                                const u16* __restrict__ xB, const u16* __restrict__ wB,
                                                u16* __restrict__ Qs, u16* __restrict__ Kp, u16* __restrict__ Vt) {
  extern __shared__ __attribute__((aligned(16))) char smem[];
  const int b = blockIdx.x, xg = b & 7, idx = b >> 3, p = xg & 1, mt = (xg >> 1) * 4 + (idx >> 3), nt = idx & 7;
  const u16* X = p ? xB : xA;
  const u16* W = p ? wB : wA;
  const int tid = threadIdx.x, w = __builtin_amdgcn_readfirstlane(tid >> 6), lane = tid & 63, r = lane & 31, h = lane >> 5;
  const int wm = w & 3, wn = w >> 2;
  int a_goff[2], a_loff[2], b_goff[3], b_loff[3];
#pragma unroll
  for (int i = 0; i < 2; ++i) {
    int c = tid + 512 * i, row = c >> 3, ch = c & 7;
    a_goff[i] = (mt * 128 + row) * 512 + ch * 8;
    a_loff[i] = row * 128 + ((ch ^ (row & 7)) << 4);
  }
#pragma unroll
  for (int i = 0; i < 3; ++i) {
    int c = tid + 512 * i, row = c / 24, ch = c % 24;
    b_goff[i] = row * 1536 + nt * 192 + ch * 8;
    b_loff[i] = G1_AS + row * G1_BSTR + ch * 16;
  }
  bf16x8 ra[2], rb[3];
#pragma unroll
  for (int i = 0; i < 2; ++i) ra[i] = *(const bf16x8*)(X + a_goff[i]);
#pragma unroll
  for (int i = 0; i < 3; ++i) rb[i] = *(const bf16x8*)(W + b_goff[i]);
#pragma unroll
  for (int i = 0; i < 2; ++i) *(bf16x8*)(smem + a_loff[i]) = ra[i];
#pragma unroll
  for (int i = 0; i < 3; ++i) *(bf16x8*)(smem + b_loff[i]) = rb[i];
  __syncthreads();

  f32x16 acc[3];
#pragma unroll
  for (int j = 0; j < 3; ++j)
#pragma unroll
    for (int i = 0; i < 16; ++i) acc[j][i] = 0.f;
  const int gcol0 = nt * 192 + wn * 96;
  const bool isV0 = gcol0 >= 1024, isV1 = gcol0 + 32 >= 1024, isV2 = gcol0 + 64 >= 1024;
  const int q4 = (lane & 15) >> 2, p4 = lane & 3, blk = (lane >> 4) & 1;
  const int a_rd = (wm * 32 + r) * 128;
  const int b_rd = G1_AS + (8 * h + q4) * G1_BSTR + (wn * 96 + 16 * blk + 4 * p4) * 2;

  for (int kt = 0; kt < 8; ++kt) {
    if (kt < 7) {
#pragma unroll
      for (int i = 0; i < 2; ++i) ra[i] = *(const bf16x8*)(X + a_goff[i] + (kt + 1) * 64);
#pragma unroll
      for (int i = 0; i < 3; ++i) rb[i] = *(const bf16x8*)(W + b_goff[i] + (kt + 1) * 64 * 1536);
    }
    const char* sb = smem + (kt & 1) * G1_BUF;
#pragma unroll
    for (int ks = 0; ks < 4; ++ks) {
      bf16x8 af = *(const bf16x8*)(sb + a_rd + (((2 * ks + h) ^ (r & 7)) << 4));
      bf16x8 bfr[3];
#pragma unroll
      for (int j = 0; j < 3; ++j) {
        const char* pb = sb + b_rd + ks * 16 * G1_BSTR + j * 64;
        bfr[j] = cat44(tr_read(pb), tr_read(pb + 4 * G1_BSTR));
      }
      if (isV0) acc[0] = MFMA32(af, bfr[0], acc[0]); else acc[0] = MFMA32(bfr[0], af, acc[0]);
      if (isV1) acc[1] = MFMA32(af, bfr[1], acc[1]); else acc[1] = MFMA32(bfr[1], af, acc[1]);
      if (isV2) acc[2] = MFMA32(af, bfr[2], acc[2]); else acc[2] = MFMA32(bfr[2], af, acc[2]);
    }
    if (kt < 7) {
      char* sn = smem + ((kt + 1) & 1) * G1_BUF;
#pragma unroll
      for (int i = 0; i < 2; ++i) *(bf16x8*)(sn + a_loff[i]) = ra[i];
#pragma unroll
      for (int i = 0; i < 3; ++i) *(bf16x8*)(sn + b_loff[i]) = rb[i];
    }
    __syncthreads();
  }
#pragma unroll
  for (int j = 0; j < 3; ++j) {
    const int gc = gcol0 + 32 * j;
    if (gc >= 1024) {
      const int n = gc - 1024 + r, hh = n >> 4, d = n & 15;
      u16* dst = Vt + ((size_t)(p * 32 + hh) * 16 + d) * 2048 + mt * 128 + wm * 32 + 4 * h;
#pragma unroll
      for (int g = 0; g < 4; ++g) {
        u32x2 v = {pk2(acc[j][4 * g], acc[j][4 * g + 1]), pk2(acc[j][4 * g + 2], acc[j][4 * g + 3])};
        *(u32x2*)(dst + 8 * g) = v;
      }
    } else {
      const bool isQ = gc < 512;
      const float sc = (isQ && p) ? QSCALE : 1.f;
      u16* base = isQ ? Qs : Kp;
      const int s = mt * 128 + wm * 32 + r;
#pragma unroll
      for (int g = 0; g < 4; ++g) {
        const int n = (gc & 511) + 8 * g + 4 * h, hh = n >> 4, d = n & 15;
        u32x2 v = {pk2(acc[j][4 * g] * sc, acc[j][4 * g + 1] * sc), pk2(acc[j][4 * g + 2] * sc, acc[j][4 * g + 3] * sc)};
        *(u32x2*)(base + ((size_t)(p * 32 + hh) * 2048 + s) * 16 + d) = v;
      }
    }
  }
}

constexpr int G3_AS = 64 * 128;
constexpr int G3_BSTR = 320;
constexpr int G3_BS = 64 * G3_BSTR;
constexpr int G3_BUF = G3_AS + G3_BS;
constexpr int G3_LDS = 2 * G3_BUF;

__global__ __launch_bounds__(512) void out_gemm(const u16* __restrict__ Oc, const u16* __restrict__ woA,
                                                const u16* __restrict__ woB, u16* __restrict__ out) {
  extern __shared__ __attribute__((aligned(16))) char smem[];
  const int b = blockIdx.x, xg = b & 7, idx = b >> 3, p = xg & 1, mt = (xg >> 1) * 8 + (idx >> 2), nt = idx & 3;
  const u16* X = Oc + (size_t)p * 2048 * 512;
  const u16* W = p ? woB : woA;
  const int tid = threadIdx.x, w = __builtin_amdgcn_readfirstlane(tid >> 6), lane = tid & 63, r = lane & 31, h = lane >> 5;
  const int wm = w & 1, wn = w >> 1;
  int a_goff, a_loff, b_goff[2], b_loff[2];
  {
    int row = tid >> 3, ch = tid & 7;
    a_goff = (mt * 64 + row) * 512 + ch * 8;
    a_loff = row * 128 + ((ch ^ (row & 7)) << 4);
  }
#pragma unroll
  for (int i = 0; i < 2; ++i) {
    int c = tid + 512 * i, row = c >> 4, ch = c & 15;
    b_goff[i] = row * 512 + nt * 128 + ch * 8;
    b_loff[i] = G3_AS + row * G3_BSTR + ch * 16;
  }
  bf16x8 ra, rb[2];
  ra = *(const bf16x8*)(X + a_goff);
#pragma unroll
  for (int i = 0; i < 2; ++i) rb[i] = *(const bf16x8*)(W + b_goff[i]);
  *(bf16x8*)(smem + a_loff) = ra;
#pragma unroll
  for (int i = 0; i < 2; ++i) *(bf16x8*)(smem + b_loff[i]) = rb[i];
  __syncthreads();
  f32x16 acc;
#pragma unroll
  for (int i = 0; i < 16; ++i) acc[i] = 0.f;
  const int q4 = (lane & 15) >> 2, p4 = lane & 3, blk = (lane >> 4) & 1;
  const int a_rd = (wm * 32 + r) * 128;
  const int b_rd = G3_AS + (8 * h + q4) * G3_BSTR + (wn * 32 + 16 * blk + 4 * p4) * 2;
  for (int kt = 0; kt < 8; ++kt) {
    if (kt < 7) {
      ra = *(const bf16x8*)(X + a_goff + (kt + 1) * 64);
#pragma unroll
      for (int i = 0; i < 2; ++i) rb[i] = *(const bf16x8*)(W + b_goff[i] + (kt + 1) * 64 * 512);
    }
    const char* sb = smem + (kt & 1) * G3_BUF;
#pragma unroll
    for (int ks = 0; ks < 4; ++ks) {
      bf16x8 af = *(const bf16x8*)(sb + a_rd + (((2 * ks + h) ^ (r & 7)) << 4));
      const char* pb = sb + b_rd + ks * 16 * G3_BSTR;
      bf16x8 bfr = cat44(tr_read(pb), tr_read(pb + 4 * G3_BSTR));
      acc = MFMA32(bfr, af, acc);
    }
    if (kt < 7) {
      char* sn = smem + ((kt + 1) & 1) * G3_BUF;
      *(bf16x8*)(sn + a_loff) = ra;
#pragma unroll
      for (int i = 0; i < 2; ++i) *(bf16x8*)(sn + b_loff[i]) = rb[i];
    }
    __syncthreads();
  }
  const int s = mt * 64 + wm * 32 + r;
  u16* dst = out + (size_t)s * 1024 + p * 512 + nt * 128 + wn * 32 + 4 * h;
#pragma unroll
  for (int g = 0; g < 4; ++g) {
    u32x2 v = {pk2(acc[4 * g], acc[4 * g + 1]), pk2(acc[4 * g + 2], acc[4 * g + 3])};
    *(u32x2*)(dst + 8 * g) = v;
  }
}

constexpr int AT_VSTR = 4112;
constexpr int AT_K = 0, AT_V = 65536;
constexpr int AT_LDS = 65536 + 16 * AT_VSTR;

DI float max16(const f32x16& s) {
  float a = fmaxf(fmaxf(s[0], s[1]), s[2]);
  float b = fmaxf(fmaxf(s[3], s[4]), s[5]);
  float c = fmaxf(fmaxf(s[6], s[7]), s[8]);
  float d = fmaxf(fmaxf(s[9], s[10]), s[11]);
  float e = fmaxf(fmaxf(s[12], s[13]), s[14]);
  a = fmaxf(fmaxf(a, b), c);
  d = fmaxf(fmaxf(d, e), s[15]);
  return fmaxf(a, d);
}

DI void attn_tile(f32x16 S, const bf16x8& vf0, const bf16x8& vf1, f32x16& negm, f32x16& O, bool first, f32x16& Sn) {
  float mx = max16(S);
  if (first || __any(mx > THR)) {
    float mo = __shfl_xor(mx, 32);
    float delta = fmaxf(mx, mo);
    if (!first) delta = fmaxf(delta, 0.f);
#pragma unroll
    for (int i = 0; i < 16; ++i) { S[i] -= delta; negm[i] -= delta; Sn[i] -= delta; }
    if (!first) {
      float alpha = __builtin_amdgcn_exp2f(-delta);
#pragma unroll
      for (int i = 0; i < 16; ++i) O[i] *= alpha;
    }
  }
#pragma unroll
  for (int i = 0; i < 16; ++i) S[i] = __builtin_amdgcn_exp2f(S[i]);
  u32x4 p0 = {pk2(S[0], S[1]), pk2(S[2], S[3]), pk2(S[4], S[5]), pk2(S[6], S[7])};
  u32x4 p1 = {pk2(S[8], S[9]), pk2(S[10], S[11]), pk2(S[12], S[13]), pk2(S[14], S[15])};
  O = MFMA32(vf0, __builtin_bit_cast(bf16x8, p0), O);
  O = MFMA32(vf1, __builtin_bit_cast(bf16x8, p1), O);
}

DI void attn_finish(const f32x16& O, u16* dst  , int r, int h) {
  float l = __shfl(O[8], r);
  float inv = 1.f / l;
  u32x2 v0 = {pk2(O[0] * inv, O[1] * inv), pk2(O[2] * inv, O[3] * inv)};
  u32x2 v1 = {pk2(O[4] * inv, O[5] * inv), pk2(O[6] * inv, O[7] * inv)};
  *(u32x2*)(dst + 4 * h) = v0;
  *(u32x2*)(dst + 8 + 4 * h) = v1;
}

__global__ __launch_bounds__(512) void attn_kernel(const u16* __restrict__ Qs, const u16* __restrict__ Kp,
                                                   const u16* __restrict__ Vt, u16* __restrict__ Oc) {
  extern __shared__ __attribute__((aligned(16))) char smem[];
  const int b = blockIdx.x, xg = b & 7, idx = b >> 3;
  const int head = xg * 4 + (idx & 3), chunk = idx >> 2;
  const int tid = threadIdx.x, w = __builtin_amdgcn_readfirstlane(tid >> 6), lane = tid & 63, r = lane & 31, h = lane >> 5;
  const int q0 = chunk * 256 + w * 32;
  const int pr = (r & ~0xC) | ((r & 4) << 1) | ((r & 8) >> 1);

  {
    const u16* Kg = Kp + (size_t)(32 + head) * 2048 * 16;
    const u16* Vg = Vt + (size_t)(32 + head) * 16 * 2048;
#pragma unroll
    for (int i = 0; i < 8; ++i) {
      const int pc = w * 8 + i;
      __builtin_amdgcn_global_load_lds((const void*)(Kg + pc * 512 + lane * 8),
                                       (__attribute__((address_space(3))) void*)(smem + AT_K + pc * 1024), 16, 0, 0);
    }
#pragma unroll
    for (int i = 0; i < 8; ++i) {
      const int pc = w * 8 + i, d = pc >> 2, part = pc & 3;
      __builtin_amdgcn_global_load_lds((const void*)(Vg + d * 2048 + part * 512 + lane * 8),
                                       (__attribute__((address_space(3))) void*)(smem + AT_V + d * AT_VSTR + part * 1024), 16, 0, 0);
    }
    if (tid == 0) {
      u32x4 ones = {0x3f803f80u, 0x3f803f80u, 0x3f803f80u, 0x3f803f80u};
      u32x4 zeros = {0u, 0u, 0u, 0u};
      *(u32x4*)(smem + AT_V + 4096) = ones;
      *(u32x4*)(smem + AT_V + AT_VSTR + 4096) = zeros;
    }
  }

  f32x16 negm, O;
  {
    const u16* Kg = Kp + (size_t)head * 2048 * 16;
    const u16* Vg = Vt + (size_t)head * 16 * 2048;
    const bf16x8 qf = *(const bf16x8*)(Qs + ((size_t)head * 2048 + q0 + r) * 16 + 8 * h);
#pragma unroll
    for (int i = 0; i < 16; ++i) { negm[i] = 0.f; O[i] = 0.f; }
    const short cval = (r == 16) ? (short)0x3f80 : (short)0;
    const bf16x8 vconst = {cval, cval, cval, cval, cval, cval, cval, cval};
    const int dd = r & 15;
    const int tmin = q0 >= 256 ? 0 : (256 - q0) >> 5;
    bf16x8 kf[9], v0[9], v1[9];
#pragma unroll
    for (int t = 0; t < 9; ++t) {
      const int key0 = max(q0 - 256 + 32 * t, 0);
      kf[t] = *(const bf16x8*)(Kg + (size_t)(key0 + pr) * 16 + 8 * h);
      v0[t] = *(const bf16x8*)(Vg + dd * 2048 + key0 + 8 * h);
      v1[t] = *(const bf16x8*)(Vg + dd * 2048 + key0 + 16 + 8 * h);
    }
    f32x16 dummy, dz;
#pragma unroll
    for (int i = 0; i < 16; ++i) dz[i] = 0.f;
#pragma unroll
    for (int t = 8; t >= 0; --t) {
      if (t >= tmin) {
        if (r >= 16) { v0[t] = vconst; v1[t] = vconst; }
        f32x16 S = MFMA32(kf[t], qf, dz);
#pragma unroll
        for (int i = 0; i < 16; i += 2) {
          const unsigned u = pk2(S[i], S[i + 1]);
          S[i] = fmaf(__uint_as_float(u << 16), QSCALE, negm[i]);
          S[i + 1] = fmaf(__uint_as_float(u & 0xffff0000u), QSCALE, negm[i + 1]);
        }
        if (t == 8 || t == 0) {
#pragma unroll
          for (int i = 0; i < 16; ++i) {
            const int ko = (i & 3) + 4 * ((i >> 2) & 1) + 8 * h + 16 * (i >> 3);
            const bool valid = (t == 8) ? (ko <= r) : (ko > r);
            S[i] = valid ? S[i] : -INFINITY;
          }
        }
        attn_tile(S, v0[t], v1[t], negm, O, t == 8, dummy);
      }
    }
    attn_finish(O, Oc + (size_t)(q0 + r) * 512 + head * 16, r, h);
  }

  __builtin_amdgcn_s_waitcnt(0x0f70);
  __syncthreads();
  {
    const bf16x8 qf = *(const bf16x8*)(Qs + ((size_t)(32 + head) * 2048 + q0 + r) * 16 + 8 * h);
#pragma unroll
    for (int i = 0; i < 16; ++i) { negm[i] = 0.f; O[i] = 0.f; }
    const char* kp = smem + AT_K + pr * 32 + 16 * h;
    int va0, va1, vinc;
    if (r < 16) { va0 = AT_V + r * AT_VSTR + 16 * h; va1 = va0 + 32; vinc = 64; }
    else { va0 = AT_V + (r == 16 ? 4096 : AT_VSTR + 4096); va1 = va0; vinc = 0; }
    {
      int a0 = va0, a1 = va1;
      f32x16 S = MFMA32(*(const bf16x8*)(kp), qf, negm);
      float mx = max16(S);
      mx = fmaxf(mx, __shfl_xor(mx, 32));
#pragma unroll
      for (int i = 0; i < 16; ++i) negm[i] = -mx;
#pragma unroll 4
      for (int t = 0; t < 64; ++t) {
        bf16x8 kf = *(const bf16x8*)(kp + t * 1024);
        bf16x8 v0 = *(const bf16x8*)(smem + a0);
        bf16x8 v1 = *(const bf16x8*)(smem + a1);
        a0 += vinc; a1 += vinc;
        S = MFMA32(kf, qf, negm);
#pragma unroll
        for (int i = 0; i < 16; ++i) S[i] = __builtin_amdgcn_exp2f(S[i]);
        u32x4 p0 = {pk2(S[0], S[1]), pk2(S[2], S[3]), pk2(S[4], S[5]), pk2(S[6], S[7])};
        u32x4 p1 = {pk2(S[8], S[9]), pk2(S[10], S[11]), pk2(S[12], S[13]), pk2(S[14], S[15])};
        O = MFMA32(v0, __builtin_bit_cast(bf16x8, p0), O);
        O = MFMA32(v1, __builtin_bit_cast(bf16x8, p1), O);
      }
    }
    bool bad = false;
#pragma unroll
    for (int i = 0; i < 9; ++i) bad |= !(fabsf(O[i]) < 1e37f);
    if (__any(bad)) {
#pragma unroll
      for (int i = 0; i < 16; ++i) { negm[i] = 0.f; O[i] = 0.f; }
      f32x16 dummy;
      for (int t = 0; t < 64; ++t) {
        bf16x8 v0 = *(const bf16x8*)(smem + va0);
        bf16x8 v1 = *(const bf16x8*)(smem + va1);
        va0 += vinc; va1 += vinc;
        f32x16 S = MFMA32(*(const bf16x8*)(kp + t * 1024), qf, negm);
        attn_tile(S, v0, v1, negm, O, t == 0, dummy);
      }
    }
    attn_finish(O, Oc + (size_t)(2048 + q0 + r) * 512 + head * 16, r, h);
  }
}

extern "C" void kernel_launch(void* const* d_in, const int* in_sizes, int n_in, void* d_out, int out_size, void* d_ws,
                              size_t ws_size, hipStream_t stream) {
  const u16* Ax = (const u16*)d_in[0];
  const u16* Awqkv = (const u16*)d_in[1];
  const u16* Awo = (const u16*)d_in[2];
  const u16* Bx = (const u16*)d_in[3];
  const u16* Bwqkv = (const u16*)d_in[4];
  const u16* Bwo = (const u16*)d_in[5];
  char* ws = (char*)d_ws;
  u16* Qs = (u16*)(ws);
  u16* Kp = (u16*)(ws + (4 << 20));
  u16* Vt = (u16*)(ws + (8 << 20));
  u16* Oc = (u16*)(ws + (12 << 20));
  (void)hipFuncSetAttribute((const void*)qkv_gemm, hipFuncAttributeMaxDynamicSharedMemorySize, G1_LDS);
  (void)hipFuncSetAttribute((const void*)attn_kernel, hipFuncAttributeMaxDynamicSharedMemorySize, AT_LDS);
  (void)hipFuncSetAttribute((const void*)out_gemm, hipFuncAttributeMaxDynamicSharedMemorySize, G3_LDS);
  qkv_gemm<<<256, 512, G1_LDS, stream>>>(Ax, Awqkv, Bx, Bwqkv, Qs, Kp, Vt);
  attn_kernel<<<256, 512, AT_LDS, stream>>>(Qs, Kp, Vt, Oc);
  out_gemm<<<256, 512, G3_LDS, stream>>>(Oc, Awo, Bwo, (u16*)d_out);
}
```

```cpp
#include <hip/hip_runtime.h>
#include <hip/hip_bf16.h>
#include <stdint.h>

typedef __attribute__((ext_vector_type(8))) short bf16x8;
typedef __attribute__((ext_vector_type(4))) short s16x4;
typedef __attribute__((ext_vector_type(16))) float f32x16;
typedef __attribute__((ext_vector_type(4))) unsigned u32x4;
typedef __attribute__((ext_vector_type(2))) unsigned u32x2;
typedef __attribute__((ext_vector_type(2))) float f32x2;
typedef __attribute__((ext_vector_type(2))) __bf16 bf16x2_t;
typedef unsigned short u16;
typedef __attribute__((address_space(3))) s16x4* lds_s16x4_p;

#define DI __device__ __forceinline__
#define MFMA32(a, b, c) __builtin_amdgcn_mfma_f32_32x32x16_bf16((a), (b), (c), 0, 0, 0)

constexpr int S_ = 2048, D_ = 512, H_ = 32, DH = 16;
constexpr float QSCALE = 0.25f * 1.4426950408889634f;
constexpr float THR = 12.f;

DI unsigned pk2(float a, float b) {
  f32x2 v = {a, b};
  bf16x2_t r = __builtin_convertvector(v, bf16x2_t);
  return __builtin_bit_cast(unsigned, r);
}
DI s16x4 tr_read(const char* p) { return __builtin_amdgcn_ds_read_tr16_b64_v4i16((lds_s16x4_p)p); }
DI bf16x8 cat44(s16x4 lo, s16x4 hi) { return __builtin_shufflevector(lo, hi, 0, 1, 2, 3, 4, 5, 6, 7); }

constexpr int G1_AS = 128 * 128;
constexpr int G1_BSTR = 448;
constexpr int G1_BS = 64 * G1_BSTR;
constexpr int G1_BUF = G1_AS + G1_BS;
constexpr int G1_LDS = 2 * G1_BUF;

__global__ __launch_bounds__(512) void qkv_gemm(const u16* __restrict__ xA, const u16* __restrict__ wA,
                                                const u16* __restrict__ xB, const u16* __restrict__ wB,
                                                u16* __restrict__ Qs, u16* __restrict__ Kp, u16* __restrict__ Vt) {
  extern __shared__ __attribute__((aligned(16))) char smem[];
  const int b = blockIdx.x, xg = b & 7, idx = b >> 3, p = xg & 1, mt = (xg >> 1) * 4 + (idx >> 3), nt = idx & 7;
  const u16* X = p ? xB : xA;
  const u16* W = p ? wB : wA;
  const int tid = threadIdx.x, w = __builtin_amdgcn_readfirstlane(tid >> 6), lane = tid & 63, r = lane & 31, h = lane >> 5;
  const int wm = w & 3, wn = w >> 2;
  int a_goff[2], a_loff[2], b_goff[3], b_loff[3];
#pragma unroll
  for (int i = 0; i < 2; ++i) {
    int c = tid + 512 * i, row = c >> 3, ch = c & 7;
    a_goff[i] = (mt * 128 + row) * 512 + ch * 8;
    a_loff[i] = row * 128 + ((ch ^ (row & 7)) << 4);
  }
#pragma unroll
  for (int i = 0; i < 3; ++i) {
    int c = tid + 512 * i, row = c / 24, ch = c % 24;
    b_goff[i] = row * 1536 + nt * 192 + ch * 8;
    b_loff[i] = G1_AS + row * G1_BSTR + ch * 16;
  }
  bf16x8 RA[8][2], RB[8][3];
#pragma unroll
  for (int kt = 0; kt < 8; ++kt) {
#pragma unroll
    for (int i = 0; i < 2; ++i) RA[kt][i] = *(const bf16x8*)(X + a_goff[i] + kt * 64);
#pragma unroll
    for (int i = 0; i < 3; ++i) RB[kt][i] = *(const bf16x8*)(W + b_goff[i] + kt * 64 * 1536);
  }
#pragma unroll
  for (int i = 0; i < 2; ++i) *(bf16x8*)(smem + a_loff[i]) = RA[0][i];
#pragma unroll
  for (int i = 0; i < 3; ++i) *(bf16x8*)(smem + b_loff[i]) = RB[0][i];
  __syncthreads();

  f32x16 acc[3];
#pragma unroll
  for (int j = 0; j < 3; ++j)
#pragma unroll
    for (int i = 0; i < 16; ++i) acc[j][i] = 0.f;
  const int gcol0 = nt * 192 + wn * 96;
  const int q4 = (lane & 15) >> 2, p4 = lane & 3, blk = (lane >> 4) & 1;
  const int a_rd = (wm * 32 + r) * 128;
  const int b_rd = G1_AS + (8 * h + q4) * G1_BSTR + (wn * 96 + 16 * blk + 4 * p4) * 2;

#pragma unroll
  for (int kt = 0; kt < 8; ++kt) {
    const char* sb = smem + (kt & 1) * G1_BUF;
#pragma unroll
    for (int ks = 0; ks < 4; ++ks) {
      bf16x8 af = *(const bf16x8*)(sb + a_rd + (((2 * ks + h) ^ (r & 7)) << 4));
      bf16x8 bfr[3];
#pragma unroll
      for (int j = 0; j < 3; ++j) {
        const char* pb = sb + b_rd + ks * 16 * G1_BSTR + j * 64;
        bfr[j] = cat44(tr_read(pb), tr_read(pb + 4 * G1_BSTR));
      }
#pragma unroll
      for (int j = 0; j < 3; ++j) acc[j] = MFMA32(bfr[j], af, acc[j]);
    }
    if (kt < 7) {
      char* sn = smem + ((kt + 1) & 1) * G1_BUF;
#pragma unroll
      for (int i = 0; i < 2; ++i) *(bf16x8*)(sn + a_loff[i]) = RA[(kt + 1) & 7][i];
#pragma unroll
      for (int i = 0; i < 3; ++i) *(bf16x8*)(sn + b_loff[i]) = RB[(kt + 1) & 7][i];
    }
    __syncthreads();
  }
#pragma unroll
  for (int j = 0; j < 3; ++j) {
    const int gc = gcol0 + 32 * j, sec = gc >> 9;
    const float sc = (sec == 0 && p) ? QSCALE : 1.f;
    u16* base = sec == 0 ? Qs : (sec == 1 ? Kp : Vt);
    const int s = mt * 128 + wm * 32 + r;
#pragma unroll
    for (int hd = 0; hd < 2; ++hd) {
      unsigned a0 = pk2(acc[j][8 * hd] * sc, acc[j][8 * hd + 1] * sc), a1 = pk2(acc[j][8 * hd + 2] * sc, acc[j][8 * hd + 3] * sc);
      unsigned b0 = pk2(acc[j][8 * hd + 4] * sc, acc[j][8 * hd + 5] * sc), b1 = pk2(acc[j][8 * hd + 6] * sc, acc[j][8 * hd + 7] * sc);
      auto r0 = __builtin_amdgcn_permlane32_swap(a0, b0, false, false);
      auto r1 = __builtin_amdgcn_permlane32_swap(a1, b1, false, false);
      u32x4 v = {r0[0], r1[0], r0[1], r1[1]};
      const int hh = ((gc & 511) >> 4) + hd;
      *(u32x4*)(base + ((size_t)(p * 32 + hh) * 2048 + s) * 16 + 8 * h) = v;
    }
  }
}

constexpr int G3_AS = 64 * 128;
constexpr int G3_BSTR = 320;
constexpr int G3_BS = 64 * G3_BSTR;
constexpr int G3_BUF = G3_AS + G3_BS;
constexpr int G3_LDS = 2 * G3_BUF;

__global__ __launch_bounds__(512) void out_gemm(const u16* __restrict__ Oc, const u16* __restrict__ woA,
                                                const u16* __restrict__ woB, u16* __restrict__ out) {
  extern __shared__ __attribute__((aligned(16))) char smem[];
  const int b = blockIdx.x, xg = b & 7, idx = b >> 3, p = xg & 1, mt = (xg >> 1) * 8 + (idx >> 2), nt = idx & 3;
  const u16* X = Oc + (size_t)p * 2048 * 512;
  const u16* W = p ? woB : woA;
  const int tid = threadIdx.x, w = __builtin_amdgcn_readfirstlane(tid >> 6), lane = tid & 63, r = lane & 31, h = lane >> 5;
  const int wm = w & 1, wn = w >> 1;
  int a_goff, a_loff, b_goff[2], b_loff[2];
  {
    int row = tid >> 3, ch = tid & 7;
    a_goff = (mt * 64 + row) * 512 + ch * 8;
    a_loff = row * 128 + ((ch ^ (row & 7)) << 4);
  }
#pragma unroll
  for (int i = 0; i < 2; ++i) {
    int c = tid + 512 * i, row = c >> 4, ch = c & 15;
    b_goff[i] = row * 512 + nt * 128 + ch * 8;
    b_loff[i] = G3_AS + row * G3_BSTR + ch * 16;
  }
  bf16x8 RA[8], RB[8][2];
#pragma unroll
  for (int kt = 0; kt < 8; ++kt) {
    RA[kt] = *(const bf16x8*)(X + a_goff + kt * 64);
#pragma unroll
    for (int i = 0; i < 2; ++i) RB[kt][i] = *(const bf16x8*)(W + b_goff[i] + kt * 64 * 512);
  }
  *(bf16x8*)(smem + a_loff) = RA[0];
#pragma unroll
  for (int i = 0; i < 2; ++i) *(bf16x8*)(smem + b_loff[i]) = RB[0][i];
  __syncthreads();
  f32x16 acc;
#pragma unroll
  for (int i = 0; i < 16; ++i) acc[i] = 0.f;
  const int q4 = (lane & 15) >> 2, p4 = lane & 3, blk = (lane >> 4) & 1;
  const int a_rd = (wm * 32 + r) * 128;
  const int b_rd = G3_AS + (8 * h + q4) * G3_BSTR + (wn * 32 + 16 * blk + 4 * p4) * 2;
#pragma unroll
  for (int kt = 0; kt < 8; ++kt) {
    const char* sb = smem + (kt & 1) * G3_BUF;
#pragma unroll
    for (int ks = 0; ks < 4; ++ks) {
      bf16x8 af = *(const bf16x8*)(sb + a_rd + (((2 * ks + h) ^ (r & 7)) << 4));
      const char* pb = sb + b_rd + ks * 16 * G3_BSTR;
      bf16x8 bfr = cat44(tr_read(pb), tr_read(pb + 4 * G3_BSTR));
      acc = MFMA32(bfr, af, acc);
    }
    if (kt < 7) {
      char* sn = smem + ((kt + 1) & 1) * G3_BUF;
      *(bf16x8*)(sn + a_loff) = RA[(kt + 1) & 7];
#pragma unroll
      for (int i = 0; i < 2; ++i) *(bf16x8*)(sn + b_loff[i]) = RB[(kt + 1) & 7][i];
    }
    __syncthreads();
  }
  const int s = mt * 64 + wm * 32 + r;
  u16* dst = out + (size_t)s * 1024 + p * 512 + nt * 128 + wn * 32 + 4 * h;
#pragma unroll
  for (int g = 0; g < 4; ++g) {
    u32x2 v = {pk2(acc[4 * g], acc[4 * g + 1]), pk2(acc[4 * g + 2], acc[4 * g + 3])};
    *(u32x2*)(dst + 8 * g) = v;
  }
}

constexpr int AT_VSTR = 4112;
constexpr int AT_K = 0, AT_V = 65536;
constexpr int AT_LDS = 163840;

DI float max16(const f32x16& s) {
  float a = fmaxf(fmaxf(s[0], s[1]), s[2]);
  float b = fmaxf(fmaxf(s[3], s[4]), s[5]);
  float c = fmaxf(fmaxf(s[6], s[7]), s[8]);
  float d = fmaxf(fmaxf(s[9], s[10]), s[11]);
  float e = fmaxf(fmaxf(s[12], s[13]), s[14]);
  a = fmaxf(fmaxf(a, b), c);
  d = fmaxf(fmaxf(d, e), s[15]);
  return fmaxf(a, d);
}

DI void attn_tile(f32x16 S, const bf16x8& vf0, const bf16x8& vf1, f32x16& negm, f32x16& O, bool first, f32x16& Sn) {
  float mx = max16(S);
  if (first || __any(mx > THR)) {
    float mo = __shfl_xor(mx, 32);
    float delta = fmaxf(mx, mo);
    if (!first) delta = fmaxf(delta, 0.f);
#pragma unroll
    for (int i = 0; i < 16; ++i) { S[i] -= delta; negm[i] -= delta; Sn[i] -= delta; }
    if (!first) {
      float alpha = __builtin_amdgcn_exp2f(-delta);
#pragma unroll
      for (int i = 0; i < 16; ++i) O[i] *= alpha;
    }
  }
#pragma unroll
  for (int i = 0; i < 16; ++i) S[i] = __builtin_amdgcn_exp2f(S[i]);
  u32x4 p0 = {pk2(S[0], S[1]), pk2(S[2], S[3]), pk2(S[4], S[5]), pk2(S[6], S[7])};
  u32x4 p1 = {pk2(S[8], S[9]), pk2(S[10], S[11]), pk2(S[12], S[13]), pk2(S[14], S[15])};
  O = MFMA32(vf0, __builtin_bit_cast(bf16x8, p0), O);
  O = MFMA32(vf1, __builtin_bit_cast(bf16x8, p1), O);
}

DI void attn_finish(const f32x16& O, u16* dst  , int r, int h) {
  float l = __shfl(O[8], r);
  float inv = 1.f / l;
  u32x2 v0 = {pk2(O[0] * inv, O[1] * inv), pk2(O[2] * inv, O[3] * inv)};
  u32x2 v1 = {pk2(O[4] * inv, O[5] * inv), pk2(O[6] * inv, O[7] * inv)};
  *(u32x2*)(dst + 4 * h) = v0;
  *(u32x2*)(dst + 8 + 4 * h) = v1;
}

constexpr int AT_KB = 0, AT_VB = 65536, AT_KA = 131072, AT_VA = 147456;
DI void glds16(const u16* g, char* l) {
  __builtin_amdgcn_global_load_lds((const void*)g, (__attribute__((address_space(3))) void*)l, 16, 0, 0);
}
DI void pv_tile(f32x16& S, const char* smem, int a0, int a1, int a2, int a3, f32x16& O) {
  bf16x8 v0 = cat44(tr_read(smem + a0), tr_read(smem + a1));
  bf16x8 v1 = cat44(tr_read(smem + a2), tr_read(smem + a3));
#pragma unroll
  for (int i = 0; i < 16; ++i) S[i] = __builtin_amdgcn_exp2f(S[i]);
  u32x4 p0 = {pk2(S[0], S[1]), pk2(S[2], S[3]), pk2(S[4], S[5]), pk2(S[6], S[7])};
  u32x4 p1 = {pk2(S[8], S[9]), pk2(S[10], S[11]), pk2(S[12], S[13]), pk2(S[14], S[15])};
  O = MFMA32(v0, __builtin_bit_cast(bf16x8, p0), O);
  O = MFMA32(v1, __builtin_bit_cast(bf16x8, p1), O);
}

__global__ __launch_bounds__(512) void attn_kernel(const u16* __restrict__ Qs, const u16* __restrict__ Kp,
                                                   const u16* __restrict__ Vt, u16* __restrict__ Oc) {
  extern __shared__ __attribute__((aligned(16))) char smem[];
  const int b = blockIdx.x, xg = b & 7, idx = b >> 3;
  const int head = xg * 4 + (idx & 3), chunk = idx >> 2;
  const int tid = threadIdx.x, w = __builtin_amdgcn_readfirstlane(tid >> 6), lane = tid & 63, r = lane & 31, h = lane >> 5;
  const int q0 = chunk * 256 + w * 32;
  const int pr = (r & ~0xC) | ((r & 4) << 1) | ((r & 8) >> 1);
  const int kw0 = chunk * 256 - 256;
  {
    const u16* KgA = Kp + ((size_t)head * 2048 + kw0) * 16;
    const u16* VgA = Vt + ((size_t)head * 2048 + kw0) * 16;
#pragma unroll
    for (int i = 0; i < 2; ++i) {
      const int pc = w * 2 + i;
      if (chunk > 0 || pc >= 8) {
        glds16(KgA + pc * 512 + lane * 8, smem + AT_KA + pc * 1024);
        glds16(VgA + pc * 512 + lane * 8, smem + AT_VA + pc * 1024);
      }
    }
    const u16* Kg = Kp + (size_t)(32 + head) * 2048 * 16;
    const u16* Vg = Vt + (size_t)(32 + head) * 2048 * 16;
#pragma unroll
    for (int i = 0; i < 8; ++i) {
      const int pc = w * 8 + i;
      glds16(Kg + pc * 512 + lane * 8, smem + AT_KB + pc * 1024);
      glds16(Vg + pc * 512 + lane * 8, smem + AT_VB + pc * 1024);
    }
  }
  const bf16x8 qfA = *(const bf16x8*)(Qs + ((size_t)head * 2048 + q0 + r) * 16 + 8 * h);
  const bf16x8 qfB = *(const bf16x8*)(Qs + ((size_t)(32 + head) * 2048 + q0 + r) * 16 + 8 * h);
  __builtin_amdgcn_s_waitcnt(0x0f70 | 0x4000);
  __builtin_amdgcn_s_waitcnt(0x0f70);
  __syncthreads();
  if (tid == 0) {
    u32x4 c0 = {0x00003f80u, 0u, 0u, 0u}, c1 = {0u, 0u, 0u, 0u};
    *(u32x4*)(smem + AT_VA) = c0;
    *(u32x4*)(smem + AT_VA + 16) = c1;
  }
  __syncthreads();

  const int q4 = (lane & 15) >> 2, p4 = lane & 3, blk = (lane >> 4) & 1;
  const int vlane = blk ? (AT_VA + 8 * p4) : ((8 * h + q4) * 32 + 8 * p4);
  const int vinc = blk ? 0 : 1024;
  const int vo1 = blk ? 0 : 128, vo2 = blk ? 0 : 512, vo3 = blk ? 0 : 640;
  f32x16 negm, O, dz;
#pragma unroll
  for (int i = 0; i < 16; ++i) dz[i] = 0.f;

  {
    const int tmin = q0 >= 256 ? 0 : (256 - q0) >> 5;
    const int krow0 = w * 32;
    const char* kp = smem + AT_KA + (krow0 + pr) * 32 + 16 * h;
    float mx = -INFINITY;
#pragma unroll
    for (int t = 0; t < 9; ++t) {
      if (t >= tmin) {
        f32x16 S = MFMA32(*(const bf16x8*)(kp + t * 1024), qfA, dz);
#pragma unroll
        for (int i = 0; i < 16; ++i) {
          const int ko = (i & 3) + 4 * ((i >> 2) & 1) + 8 * h + 16 * (i >> 3);
          if (t == 8) S[i] = (ko <= r) ? S[i] : -INFINITY;
          if (t == 0) S[i] = (ko > r) ? S[i] : -INFINITY;
        }
        mx = fmaxf(mx, max16(S));
      }
    }
    mx = fmaxf(mx, __shfl_xor(mx, 32));
    const float nm = -__uint_as_float(pk2(mx, mx) << 16) * QSCALE;
#pragma unroll
    for (int i = 0; i < 16; ++i) O[i] = 0.f;
#pragma unroll
    for (int t = 0; t < 9; ++t) {
      if (t >= tmin) {
        f32x16 S = MFMA32(*(const bf16x8*)(kp + t * 1024), qfA, dz);
#pragma unroll
        for (int i = 0; i < 16; i += 2) {
          const unsigned u = pk2(S[i], S[i + 1]);
          S[i] = fmaf(__uint_as_float(u << 16), QSCALE, nm);
          S[i + 1] = fmaf(__uint_as_float(u & 0xffff0000u), QSCALE, nm);
        }
#pragma unroll
        for (int i = 0; i < 16; ++i) {
          const int ko = (i & 3) + 4 * ((i >> 2) & 1) + 8 * h + 16 * (i >> 3);
          if (t == 8) S[i] = (ko <= r) ? S[i] : -INFINITY;
          if (t == 0) S[i] = (ko > r) ? S[i] : -INFINITY;
        }
        const int a = blk ? vlane : AT_VA + krow0 * 32 + t * 1024 + vlane;
        pv_tile(S, smem, a, a + vo1, a + vo2, a + vo3, O);
      }
    }
    attn_finish(O, Oc + (size_t)(q0 + r) * 512 + head * 16, r, h);
  }

  {
#pragma unroll
    for (int i = 0; i < 16; ++i) O[i] = 0.f;
    const char* kp = smem + AT_KB + pr * 32 + 16 * h;
    const int vb = blk ? vlane : AT_VB + vlane;
    {
      f32x16 S = MFMA32(*(const bf16x8*)(kp), qfB, dz);
      float mx = max16(S);
      mx = fmaxf(mx, __shfl_xor(mx, 32));
#pragma unroll
      for (int i = 0; i < 16; ++i) negm[i] = -mx;
      int a = vb;
#pragma unroll 4
      for (int t = 0; t < 64; ++t) {
        S = MFMA32(*(const bf16x8*)(kp + t * 1024), qfB, negm);
        pv_tile(S, smem, a, a + vo1, a + vo2, a + vo3, O);
        a += vinc;
      }
    }
    bool bad = false;
#pragma unroll
    for (int i = 0; i < 9; ++i) bad |= !(fabsf(O[i]) < 1e37f);
    if (__any(bad)) {
#pragma unroll
      for (int i = 0; i < 16; ++i) { negm[i] = 0.f; O[i] = 0.f; }
      f32x16 dummy;
      int a = vb;
      for (int t = 0; t < 64; ++t) {
        bf16x8 v0 = cat44(tr_read(smem + a), tr_read(smem + a + vo1));
        bf16x8 v1 = cat44(tr_read(smem + a + vo2), tr_read(smem + a + vo3));
        a += vinc;
        f32x16 S = MFMA32(*(const bf16x8*)(kp + t * 1024), qfB, negm);
        attn_tile(S, v0, v1, negm, O, t == 0, dummy);
      }
    }
    attn_finish(O, Oc + (size_t)(2048 + q0 + r) * 512 + head * 16, r, h);
  }
}

extern "C" void kernel_launch(void* const* d_in, const int* in_sizes, int n_in, void* d_out, int out_size, void* d_ws,
                              size_t ws_size, hipStream_t stream) {
  const u16* Ax = (const u16*)d_in[0];
  const u16* Awqkv = (const u16*)d_in[1];
  const u16* Awo = (const u16*)d_in[2];
  const u16* Bx = (const u16*)d_in[3];
  const u16* Bwqkv = (const u16*)d_in[4];
  const u16* Bwo = (const u16*)d_in[5];
  char* ws = (char*)d_ws;
  u16* Qs = (u16*)(ws);
  u16* Kp = (u16*)(ws + (4 << 20));
  u16* Vt = (u16*)(ws + (8 << 20));
  u16* Oc = (u16*)(ws + (12 << 20));
  (void)hipFuncSetAttribute((const void*)qkv_gemm, hipFuncAttributeMaxDynamicSharedMemorySize, G1_LDS);
  (void)hipFuncSetAttribute((const void*)attn_kernel, hipFuncAttributeMaxDynamicSharedMemorySize, AT_LDS);
  (void)hipFuncSetAttribute((const void*)out_gemm, hipFuncAttributeMaxDynamicSharedMemorySize, G3_LDS);
  qkv_gemm<<<256, 512, G1_LDS, stream>>>(Ax, Awqkv, Bx, Bwqkv, Qs, Kp, Vt);
  attn_kernel<<<256, 512, AT_LDS, stream>>>(Qs, Kp, Vt, Oc);
  out_gemm<<<256, 512, G3_LDS, stream>>>(Oc, Awo, Bwo, (u16*)d_out);
}
```

```cpp
#include <hip/hip_runtime.h>
#include <hip/hip_bf16.h>
#include <stdint.h>

typedef __attribute__((ext_vector_type(8))) short bf16x8;
typedef __attribute__((ext_vector_type(4))) short s16x4;
typedef __attribute__((ext_vector_type(16))) float f32x16;
typedef __attribute__((ext_vector_type(4))) unsigned u32x4;
typedef __attribute__((ext_vector_type(2))) unsigned u32x2;
typedef __attribute__((ext_vector_type(2))) float f32x2;
typedef __attribute__((ext_vector_type(2))) __bf16 bf16x2_t;
typedef unsigned short u16;
typedef __attribute__((address_space(3))) s16x4* lds_s16x4_p;

#define DI __device__ __forceinline__
#define MFMA32(a, b, c) __builtin_amdgcn_mfma_f32_32x32x16_bf16((a), (b), (c), 0, 0, 0)

constexpr int S_ = 2048, D_ = 512, H_ = 32, DH = 16;
constexpr float QSCALE = 0.25f * 1.4426950408889634f;
constexpr float THR = 12.f;

DI unsigned pk2(float a, float b) {
  f32x2 v = {a, b};
  bf16x2_t r = __builtin_convertvector(v, bf16x2_t);
  return __builtin_bit_cast(unsigned, r);
}
DI s16x4 tr_read(const char* p) { return __builtin_amdgcn_ds_read_tr16_b64_v4i16((lds_s16x4_p)p); }
DI bf16x8 cat44(s16x4 lo, s16x4 hi) { return __builtin_shufflevector(lo, hi, 0, 1, 2, 3, 4, 5, 6, 7); }

constexpr int G1_AS = 128 * 128;
constexpr int G1_BSTR = 448;
constexpr int G1_BS = 64 * G1_BSTR;
constexpr int G1_BUF = G1_AS + G1_BS;
constexpr int G1_LDS = 2 * G1_BUF;

__global__ __launch_bounds__(512) void qkv_gemm(const u16* __restrict__ xA, const u16* __restrict__ wA,
                                                const u16* __restrict__ xB, const u16* __restrict__ wB,
                                                u16* __restrict__ Qs, u16* __restrict__ Kp, u16* __restrict__ Vt) {
  extern __shared__ __attribute__((aligned(16))) char smem[];
  const int b = blockIdx.x, xg = b & 7, idx = b >> 3, p = xg & 1, mt = (xg >> 1) * 4 + (idx >> 3), nt = idx & 7;
  const u16* X = p ? xB : xA;
  const u16* W = p ? wB : wA;
  const int tid = threadIdx.x, w = __builtin_amdgcn_readfirstlane(tid >> 6), lane = tid & 63, r = lane & 31, h = lane >> 5;
  const int wm = w & 3, wn = w >> 2;
  int a_goff[2], a_loff[2], b_goff[3], b_loff[3];
#pragma unroll
  for (int i = 0; i < 2; ++i) {
    int c = tid + 512 * i, row = c >> 3, ch = c & 7;
    a_goff[i] = (mt * 128 + row) * 512 + ch * 8;
    a_loff[i] = row * 128 + ((ch ^ (row & 7)) << 4);
  }
#pragma unroll
  for (int i = 0; i < 3; ++i) {
    int c = tid + 512 * i, row = c / 24, ch = c % 24;
    b_goff[i] = row * 1536 + nt * 192 + ch * 8;
    b_loff[i] = G1_AS + row * G1_BSTR + ch * 16;
  }
  bf16x8 RA[8][2], RB[8][3];
#pragma unroll
  for (int kt = 0; kt < 8; ++kt) {
#pragma unroll
    for (int i = 0; i < 2; ++i) RA[kt][i] = *(const bf16x8*)(X + a_goff[i] + kt * 64);
#pragma unroll
    for (int i = 0; i < 3; ++i) RB[kt][i] = *(const bf16x8*)(W + b_goff[i] + kt * 64 * 1536);
  }
#pragma unroll
  for (int i = 0; i < 2; ++i) *(bf16x8*)(smem + a_loff[i]) = RA[0][i];
#pragma unroll
  for (int i = 0; i < 3; ++i) *(bf16x8*)(smem + b_loff[i]) = RB[0][i];
  __syncthreads();

  f32x16 acc[3];
#pragma unroll
  for (int j = 0; j < 3; ++j)
#pragma unroll
    for (int i = 0; i < 16; ++i) acc[j][i] = 0.f;
  const int gcol0 = nt * 192 + wn * 96;
  const int q4 = (lane & 15) >> 2, p4 = lane & 3, blk = (lane >> 4) & 1;
  const int a_rd = (wm * 32 + r) * 128;
  const int b_rd = G1_AS + (8 * h + q4) * G1_BSTR + (wn * 96 + 16 * blk + 4 * p4) * 2;

#pragma unroll
  for (int kt = 0; kt < 8; ++kt) {
    const char* sb = smem + (kt & 1) * G1_BUF;
#pragma unroll
    for (int ks = 0; ks < 4; ++ks) {
      bf16x8 af = *(const bf16x8*)(sb + a_rd + (((2 * ks + h) ^ (r & 7)) << 4));
      bf16x8 bfr[3];
#pragma unroll
      for (int j = 0; j < 3; ++j) {
        const char* pb = sb + b_rd + ks * 16 * G1_BSTR + j * 64;
        bfr[j] = cat44(tr_read(pb), tr_read(pb + 4 * G1_BSTR));
      }
#pragma unroll
      for (int j = 0; j < 3; ++j) acc[j] = MFMA32(bfr[j], af, acc[j]);
    }
    if (kt < 7) {
      char* sn = smem + ((kt + 1) & 1) * G1_BUF;
#pragma unroll
      for (int i = 0; i < 2; ++i) *(bf16x8*)(sn + a_loff[i]) = RA[(kt + 1) & 7][i];
#pragma unroll
      for (int i = 0; i < 3; ++i) *(bf16x8*)(sn + b_loff[i]) = RB[(kt + 1) & 7][i];
    }
    __syncthreads();
  }
#pragma unroll
  for (int j = 0; j < 3; ++j) {
    const int gc = gcol0 + 32 * j, sec = gc >> 9;
    const float sc = (sec == 0 && p) ? QSCALE : 1.f;
    u16* base = sec == 0 ? Qs : (sec == 1 ? Kp : Vt);
    const int s = mt * 128 + wm * 32 + r;
#pragma unroll
    for (int hd = 0; hd < 2; ++hd) {
      unsigned a0 = pk2(acc[j][8 * hd] * sc, acc[j][8 * hd + 1] * sc), a1 = pk2(acc[j][8 * hd + 2] * sc, acc[j][8 * hd + 3] * sc);
      unsigned b0 = pk2(acc[j][8 * hd + 4] * sc, acc[j][8 * hd + 5] * sc), b1 = pk2(acc[j][8 * hd + 6] * sc, acc[j][8 * hd + 7] * sc);
      auto r0 = __builtin_amdgcn_permlane32_swap(a0, b0, false, false);
      auto r1 = __builtin_amdgcn_permlane32_swap(a1, b1, false, false);
      u32x4 v = {r0[0], r1[0], r0[1], r1[1]};
      const int hh = ((gc & 511) >> 4) + hd;
      *(u32x4*)(base + ((size_t)(p * 32 + hh) * 2048 + s) * 16 + 8 * h) = v;
    }
  }
}

constexpr int G3_AS = 64 * 128;
constexpr int G3_BSTR = 320;
constexpr int G3_BS = 64 * G3_BSTR;
constexpr int G3_BUF = G3_AS + G3_BS;
constexpr int G3_LDS = 2 * G3_BUF;

__global__ __launch_bounds__(512) void out_gemm(const u16* __restrict__ Oc, const u16* __restrict__ woA,
                                                const u16* __restrict__ woB, u16* __restrict__ out) {
  extern __shared__ __attribute__((aligned(16))) char smem[];
  const int b = blockIdx.x, xg = b & 7, idx = b >> 3, p = xg & 1, mt = (xg >> 1) * 8 + (idx >> 2), nt = idx & 3;
  const u16* X = Oc + (size_t)p * 2048 * 512;
  const u16* W = p ? woB : woA;
  const int tid = threadIdx.x, w = __builtin_amdgcn_readfirstlane(tid >> 6), lane = tid & 63, r = lane & 31, h = lane >> 5;
  const int wm = w & 1, wn = w >> 1;
  int a_goff, a_loff, b_goff[2], b_loff[2];
  {
    int row = tid >> 3, ch = tid & 7;
    a_goff = (mt * 64 + row) * 512 + ch * 8;
    a_loff = row * 128 + ((ch ^ (row & 7)) << 4);
  }
#pragma unroll
  for (int i = 0; i < 2; ++i) {
    int c = tid + 512 * i, row = c >> 4, ch = c & 15;
    b_goff[i] = row * 512 + nt * 128 + ch * 8;
    b_loff[i] = G3_AS + row * G3_BSTR + ch * 16;
  }
  bf16x8 RA[8], RB[8][2];
#pragma unroll
  for (int kt = 0; kt < 8; ++kt) {
    RA[kt] = *(const bf16x8*)(X + a_goff + kt * 64);
#pragma unroll
    for (int i = 0; i < 2; ++i) RB[kt][i] = *(const bf16x8*)(W + b_goff[i] + kt * 64 * 512);
  }
  *(bf16x8*)(smem + a_loff) = RA[0];
#pragma unroll
  for (int i = 0; i < 2; ++i) *(bf16x8*)(smem + b_loff[i]) = RB[0][i];
  __syncthreads();
  f32x16 acc;
#pragma unroll
  for (int i = 0; i < 16; ++i) acc[i] = 0.f;
  const int q4 = (lane & 15) >> 2, p4 = lane & 3, blk = (lane >> 4) & 1;
  const int a_rd = (wm * 32 + r) * 128;
  const int b_rd = G3_AS + (8 * h + q4) * G3_BSTR + (wn * 32 + 16 * blk + 4 * p4) * 2;
#pragma unroll
  for (int kt = 0; kt < 8; ++kt) {
    const char* sb = smem + (kt & 1) * G3_BUF;
#pragma unroll
    for (int ks = 0; ks < 4; ++ks) {
      bf16x8 af = *(const bf16x8*)(sb + a_rd + (((2 * ks + h) ^ (r & 7)) << 4));
      const char* pb = sb + b_rd + ks * 16 * G3_BSTR;
      bf16x8 bfr = cat44(tr_read(pb), tr_read(pb + 4 * G3_BSTR));
      acc = MFMA32(bfr, af, acc);
    }
    if (kt < 7) {
      char* sn = smem + ((kt + 1) & 1) * G3_BUF;
      *(bf16x8*)(sn + a_loff) = RA[(kt + 1) & 7];
#pragma unroll
      for (int i = 0; i < 2; ++i) *(bf16x8*)(sn + b_loff[i]) = RB[(kt + 1) & 7][i];
    }
    __syncthreads();
  }
  const int s = mt * 64 + wm * 32 + r;
  u16* dst = out + (size_t)s * 1024 + p * 512 + nt * 128 + wn * 32 + 4 * h;
#pragma unroll
  for (int g = 0; g < 4; ++g) {
    u32x2 v = {pk2(acc[4 * g], acc[4 * g + 1]), pk2(acc[4 * g + 2], acc[4 * g + 3])};
    *(u32x2*)(dst + 8 * g) = v;
  }
}

constexpr int AT_VSTR = 4112;
constexpr int AT_K = 0, AT_V = 65536;
constexpr int AT_LDS = 163840;

DI float max16(const f32x16& s) {
  float a = fmaxf(fmaxf(s[0], s[1]), s[2]);
  float b = fmaxf(fmaxf(s[3], s[4]), s[5]);
  float c = fmaxf(fmaxf(s[6], s[7]), s[8]);
  float d = fmaxf(fmaxf(s[9], s[10]), s[11]);
  float e = fmaxf(fmaxf(s[12], s[13]), s[14]);
  a = fmaxf(fmaxf(a, b), c);
  d = fmaxf(fmaxf(d, e), s[15]);
  return fmaxf(a, d);
}

DI void attn_tile(f32x16 S, const bf16x8& vf0, const bf16x8& vf1, f32x16& negm, f32x16& O, bool first, f32x16& Sn) {
  float mx = max16(S);
  if (first || __any(mx > THR)) {
    float mo = __shfl_xor(mx, 32);
    float delta = fmaxf(mx, mo);
    if (!first) delta = fmaxf(delta, 0.f);
#pragma unroll
    for (int i = 0; i < 16; ++i) { S[i] -= delta; negm[i] -= delta; Sn[i] -= delta; }
    if (!first) {
      float alpha = __builtin_amdgcn_exp2f(-delta);
#pragma unroll
      for (int i = 0; i < 16; ++i) O[i] *= alpha;
    }
  }
#pragma unroll
  for (int i = 0; i < 16; ++i) S[i] = __builtin_amdgcn_exp2f(S[i]);
  u32x4 p0 = {pk2(S[0], S[1]), pk2(S[2], S[3]), pk2(S[4], S[5]), pk2(S[6], S[7])};
  u32x4 p1 = {pk2(S[8], S[9]), pk2(S[10], S[11]), pk2(S[12], S[13]), pk2(S[14], S[15])};
  O = MFMA32(vf0, __builtin_bit_cast(bf16x8, p0), O);
  O = MFMA32(vf1, __builtin_bit_cast(bf16x8, p1), O);
}

DI void attn_finish(const f32x16& O, u16* dst  , int r, int h) {
  float l = __shfl(O[8], r);
  float inv = 1.f / l;
  u32x2 v0 = {pk2(O[0] * inv, O[1] * inv), pk2(O[2] * inv, O[3] * inv)};
  u32x2 v1 = {pk2(O[4] * inv, O[5] * inv), pk2(O[6] * inv, O[7] * inv)};
  *(u32x2*)(dst + 4 * h) = v0;
  *(u32x2*)(dst + 8 + 4 * h) = v1;
}

constexpr int AT_KB = 0, AT_VB = 65536, AT_KA = 131072, AT_VA = 147456;
DI void glds16(const u16* g, char* l) {
  __builtin_amdgcn_global_load_lds((const void*)g, (__attribute__((address_space(3))) void*)l, 16, 0, 0);
}
DI void pv_tile(f32x16& S, const char* smem, int a0, int a1, int a2, int a3, f32x16& O) {
  bf16x8 v0 = cat44(tr_read(smem + a0), tr_read(smem + a1));
  bf16x8 v1 = cat44(tr_read(smem + a2), tr_read(smem + a3));
#pragma unroll
  for (int i = 0; i < 16; ++i) S[i] = __builtin_amdgcn_exp2f(S[i]);
  u32x4 p0 = {pk2(S[0], S[1]), pk2(S[2], S[3]), pk2(S[4], S[5]), pk2(S[6], S[7])};
  u32x4 p1 = {pk2(S[8], S[9]), pk2(S[10], S[11]), pk2(S[12], S[13]), pk2(S[14], S[15])};
  O = MFMA32(v0, __builtin_bit_cast(bf16x8, p0), O);
  O = MFMA32(v1, __builtin_bit_cast(bf16x8, p1), O);
}

__global__ __launch_bounds__(1024) void attn_kernel(const u16* __restrict__ Qs, const u16* __restrict__ Kp,
                                                   const u16* __restrict__ Vt, u16* __restrict__ Oc) {
  extern __shared__ __attribute__((aligned(16))) char smem[];
  const int b = blockIdx.x, xg = b & 7, idx = b >> 3;
  const int head = xg * 4 + (idx & 3), chunk = idx >> 2;
  const int tid = threadIdx.x, w16 = __builtin_amdgcn_readfirstlane(tid >> 6), lane = tid & 63, r = lane & 31, h = lane >> 5;
  const int w = w16 & 7, grp = w16 >> 3;
  const int q0 = chunk * 256 + w * 32;
  const int pr = (r & ~0xC) | ((r & 4) << 1) | ((r & 8) >> 1);
  const int kw0 = chunk * 256 - 256;
  {
    const u16* KgA = Kp + ((size_t)head * 2048 + kw0) * 16;
    const u16* VgA = Vt + ((size_t)head * 2048 + kw0) * 16;
#pragma unroll
    for (int i = 0; i < 1; ++i) {
      const int pc = w16;
      if (chunk > 0 || pc >= 8) {
        glds16(KgA + pc * 512 + lane * 8, smem + AT_KA + pc * 1024);
        glds16(VgA + pc * 512 + lane * 8, smem + AT_VA + pc * 1024);
      }
    }
    const u16* Kg = Kp + (size_t)(32 + head) * 2048 * 16;
    const u16* Vg = Vt + (size_t)(32 + head) * 2048 * 16;
#pragma unroll
    for (int i = 0; i < 4; ++i) {
      const int pc = w16 * 4 + i;
      glds16(Kg + pc * 512 + lane * 8, smem + AT_KB + pc * 1024);
      glds16(Vg + pc * 512 + lane * 8, smem + AT_VB + pc * 1024);
    }
  }
  const bf16x8 qfA = *(const bf16x8*)(Qs + ((size_t)head * 2048 + q0 + r) * 16 + 8 * h);
  const bf16x8 qfB = *(const bf16x8*)(Qs + ((size_t)(32 + head) * 2048 + q0 + r) * 16 + 8 * h);
  __builtin_amdgcn_s_waitcnt(0x0f70 | 0x4000);
  __builtin_amdgcn_s_waitcnt(0x0f70);
  __syncthreads();
  if (tid == 0) {
    u32x4 c0 = {0x00003f80u, 0u, 0u, 0u}, c1 = {0u, 0u, 0u, 0u};
    *(u32x4*)(smem + AT_VA) = c0;
    *(u32x4*)(smem + AT_VA + 16) = c1;
  }
  __syncthreads();

  const int q4 = (lane & 15) >> 2, p4 = lane & 3, blk = (lane >> 4) & 1;
  const int vlane = blk ? (AT_VA + 8 * p4) : ((8 * h + q4) * 32 + 8 * p4);
  const int vinc = blk ? 0 : 1024;
  const int vo1 = blk ? 0 : 128, vo2 = blk ? 0 : 512, vo3 = blk ? 0 : 640;
  f32x16 negm, O, dz;
#pragma unroll
  for (int i = 0; i < 16; ++i) dz[i] = 0.f;

  if (grp == 0) {
    const int tmin = q0 >= 256 ? 0 : (256 - q0) >> 5;
    const int krow0 = w * 32;
    const char* kp = smem + AT_KA + (krow0 + pr) * 32 + 16 * h;
    float mx = -INFINITY;
#pragma unroll
    for (int t = 0; t < 9; ++t) {
      if (t >= tmin) {
        f32x16 S = MFMA32(*(const bf16x8*)(kp + t * 1024), qfA, dz);
#pragma unroll
        for (int i = 0; i < 16; ++i) {
          const int ko = (i & 3) + 4 * ((i >> 2) & 1) + 8 * h + 16 * (i >> 3);
          if (t == 8) S[i] = (ko <= r) ? S[i] : -INFINITY;
          if (t == 0) S[i] = (ko > r) ? S[i] : -INFINITY;
        }
        mx = fmaxf(mx, max16(S));
      }
    }
    mx = fmaxf(mx, __shfl_xor(mx, 32));
    const float nm = -__uint_as_float(pk2(mx, mx) << 16) * QSCALE;
#pragma unroll
    for (int i = 0; i < 16; ++i) O[i] = 0.f;
#pragma unroll
    for (int t = 0; t < 9; ++t) {
      if (t >= tmin) {
        f32x16 S = MFMA32(*(const bf16x8*)(kp + t * 1024), qfA, dz);
#pragma unroll
        for (int i = 0; i < 16; i += 2) {
          const unsigned u = pk2(S[i], S[i + 1]);
          S[i] = fmaf(__uint_as_float(u << 16), QSCALE, nm);
          S[i + 1] = fmaf(__uint_as_float(u & 0xffff0000u), QSCALE, nm);
        }
#pragma unroll
        for (int i = 0; i < 16; ++i) {
          const int ko = (i & 3) + 4 * ((i >> 2) & 1) + 8 * h + 16 * (i >> 3);
          if (t == 8) S[i] = (ko <= r) ? S[i] : -INFINITY;
          if (t == 0) S[i] = (ko > r) ? S[i] : -INFINITY;
        }
        const int a = blk ? vlane : AT_VA + krow0 * 32 + t * 1024 + vlane;
        pv_tile(S, smem, a, a + vo1, a + vo2, a + vo3, O);
      }
    }
    attn_finish(O, Oc + (size_t)(q0 + r) * 512 + head * 16, r, h);
  }

  {
#pragma unroll
    for (int i = 0; i < 16; ++i) O[i] = 0.f;
    const char* kp = smem + AT_KB + pr * 32 + 16 * h;
    const int vb = blk ? vlane : AT_VB + vlane;
    {
      f32x16 S = MFMA32(*(const bf16x8*)(kp), qfB, dz);
      float mx = max16(S);
      mx = fmaxf(mx, __shfl_xor(mx, 32));
#pragma unroll
      for (int i = 0; i < 16; ++i) negm[i] = -mx;
      const int t0 = grp ? 24 : 0, t1 = grp ? 64 : 24;
      int a = vb + t0 * vinc;
      const char* kq = kp + t0 * 1024;
#pragma unroll 4
      for (int t = t0; t < t1; ++t) {
        S = MFMA32(*(const bf16x8*)(kq), qfB, negm);
        pv_tile(S, smem, a, a + vo1, a + vo2, a + vo3, O);
        a += vinc; kq += 1024;
      }
    }
    __syncthreads();
    float* mo = (float*)(smem + AT_KA + w * 2048) + lane;
    float* ml = (float*)(smem + AT_VA + 64 + w * 256) + lane;
    if (grp == 1) {
#pragma unroll
      for (int i = 0; i < 8; ++i) mo[i * 64] = O[i];
      *ml = O[8];
    }
    __syncthreads();
    if (grp == 1) return;
#pragma unroll
    for (int i = 0; i < 8; ++i) O[i] += mo[i * 64];
    O[8] += *ml;
    bool bad = false;
#pragma unroll
    for (int i = 0; i < 9; ++i) bad |= !(fabsf(O[i]) < 1e37f);
    if (__any(bad)) {
#pragma unroll
      for (int i = 0; i < 16; ++i) { negm[i] = 0.f; O[i] = 0.f; }
      f32x16 dummy;
      int a = vb;
      for (int t = 0; t < 64; ++t) {
        bf16x8 v0 = cat44(tr_read(smem + a), tr_read(smem + a + vo1));
        bf16x8 v1 = cat44(tr_read(smem + a + vo2), tr_read(smem + a + vo3));
        a += vinc;
        f32x16 S = MFMA32(*(const bf16x8*)(kp + t * 1024), qfB, negm);
        attn_tile(S, v0, v1, negm, O, t == 0, dummy);
      }
    }
    attn_finish(O, Oc + (size_t)(2048 + q0 + r) * 512 + head * 16, r, h);
  }
}

extern "C" void kernel_launch(void* const* d_in, const int* in_sizes, int n_in, void* d_out, int out_size, void* d_ws,
                              size_t ws_size, hipStream_t stream) {
  const u16* Ax = (const u16*)d_in[0];
  const u16* Awqkv = (const u16*)d_in[1];
  const u16* Awo = (const u16*)d_in[2];
  const u16* Bx = (const u16*)d_in[3];
  const u16* Bwqkv = (const u16*)d_in[4];
  const u16* Bwo = (const u16*)d_in[5];
  char* ws = (char*)d_ws;
  u16* Qs = (u16*)(ws);
  u16* Kp = (u16*)(ws + (4 << 20));
  u16* Vt = (u16*)(ws + (8 << 20));
  u16* Oc = (u16*)(ws + (12 << 20));
  (void)hipFuncSetAttribute((const void*)qkv_gemm, hipFuncAttributeMaxDynamicSharedMemorySize, G1_LDS);
  (void)hipFuncSetAttribute((const void*)attn_kernel, hipFuncAttributeMaxDynamicSharedMemorySize, AT_LDS);
  (void)hipFuncSetAttribute((const void*)out_gemm, hipFuncAttributeMaxDynamicSharedMemorySize, G3_LDS);
  qkv_gemm<<<256, 512, G1_LDS, stream>>>(Ax, Awqkv, Bx, Bwqkv, Qs, Kp, Vt);
  attn_kernel<<<256, 1024, AT_LDS, stream>>>(Qs, Kp, Vt, Oc);
  out_gemm<<<256, 512, G3_LDS, stream>>>(Oc, Awo, Bwo, (u16*)d_out);
}
```

```cpp
#include <hip/hip_runtime.h>
#include <hip/hip_bf16.h>
#include <stdint.h>

typedef __attribute__((ext_vector_type(8))) short bf16x8;
typedef __attribute__((ext_vector_type(4))) short s16x4;
typedef __attribute__((ext_vector_type(16))) float f32x16;
typedef __attribute__((ext_vector_type(4))) unsigned u32x4;
typedef __attribute__((ext_vector_type(2))) unsigned u32x2;
typedef __attribute__((ext_vector_type(2))) float f32x2;
typedef __attribute__((ext_vector_type(2))) __bf16 bf16x2_t;
typedef unsigned short u16;
typedef __attribute__((address_space(3))) s16x4* lds_s16x4_p;

#define DI __device__ __forceinline__
#define MFMA32(a, b, c) __builtin_amdgcn_mfma_f32_32x32x16_bf16((a), (b), (c), 0, 0, 0)

constexpr int S_ = 2048, D_ = 512, H_ = 32, DH = 16;
constexpr float QSCALE = 0.25f * 1.4426950408889634f;
constexpr float THR = 12.f;

DI unsigned pk2(float a, float b) {
  f32x2 v = {a, b};
  bf16x2_t r = __builtin_convertvector(v, bf16x2_t);
  return __builtin_bit_cast(unsigned, r);
}
DI s16x4 tr_read(const char* p) { return __builtin_amdgcn_ds_read_tr16_b64_v4i16((lds_s16x4_p)p); }
DI bf16x8 cat44(s16x4 lo, s16x4 hi) { return __builtin_shufflevector(lo, hi, 0, 1, 2, 3, 4, 5, 6, 7); }

constexpr int G1_AS = 128 * 128;
constexpr int G1_BSTR = 448;
constexpr int G1_BS = 64 * G1_BSTR;
constexpr int G1_BUF = G1_AS + G1_BS;
constexpr int G1_LDS = 2 * G1_BUF;

__global__ __launch_bounds__(512) void qkv_gemm(const u16* __restrict__ xA, const u16* __restrict__ wA,
                                                const u16* __restrict__ xB, const u16* __restrict__ wB,
                                                u16* __restrict__ Qs, u16* __restrict__ Kp, u16* __restrict__ Vt) {
  extern __shared__ __attribute__((aligned(16))) char smem[];
  const int b = blockIdx.x, xg = b & 7, idx = b >> 3, p = xg & 1, mt = (xg >> 1) * 4 + (idx >> 3), nt = idx & 7;
  const u16* X = p ? xB : xA;
  const u16* W = p ? wB : wA;
  const int tid = threadIdx.x, w = __builtin_amdgcn_readfirstlane(tid >> 6), lane = tid & 63, r = lane & 31, h = lane >> 5;
  const int wm = w & 3, wn = w >> 2;
  int a_goff[2], a_loff[2], b_goff[3], b_loff[3];
#pragma unroll
  for (int i = 0; i < 2; ++i) {
    int c = tid + 512 * i, row = c >> 3, ch = c & 7;
    a_goff[i] = (mt * 128 + row) * 512 + ch * 8;
    a_loff[i] = row * 128 + ((ch ^ (row & 7)) << 4);
  }
#pragma unroll
  for (int i = 0; i < 3; ++i) {
    int c = tid + 512 * i, row = c / 24, ch = c % 24;
    b_goff[i] = row * 1536 + nt * 192 + ch * 8;
    b_loff[i] = G1_AS + row * G1_BSTR + ch * 16;
  }
  bf16x8 RA[8][2], RB[8][3];
#pragma unroll
  for (int kt = 0; kt < 8; ++kt) {
#pragma unroll
    for (int i = 0; i < 2; ++i) RA[kt][i] = *(const bf16x8*)(X + a_goff[i] + kt * 64);
#pragma unroll
    for (int i = 0; i < 3; ++i) RB[kt][i] = *(const bf16x8*)(W + b_goff[i] + kt * 64 * 1536);
  }
#pragma unroll
  for (int i = 0; i < 2; ++i) *(bf16x8*)(smem + a_loff[i]) = RA[0][i];
#pragma unroll
  for (int i = 0; i < 3; ++i) *(bf16x8*)(smem + b_loff[i]) = RB[0][i];
  __syncthreads();

  f32x16 acc[3];
#pragma unroll
  for (int j = 0; j < 3; ++j)
#pragma unroll
    for (int i = 0; i < 16; ++i) acc[j][i] = 0.f;
  const int gcol0 = nt * 192 + wn * 96;
  const int q4 = (lane & 15) >> 2, p4 = lane & 3, blk = (lane >> 4) & 1;
  const int a_rd = (wm * 32 + r) * 128;
  const int b_rd = G1_AS + (8 * h + q4) * G1_BSTR + (wn * 96 + 16 * blk + 4 * p4) * 2;

#pragma unroll
  for (int kt = 0; kt < 8; ++kt) {
    const char* sb = smem + (kt & 1) * G1_BUF;
#pragma unroll
    for (int ks = 0; ks < 4; ++ks) {
      bf16x8 af = *(const bf16x8*)(sb + a_rd + (((2 * ks + h) ^ (r & 7)) << 4));
      bf16x8 bfr[3];
#pragma unroll
      for (int j = 0; j < 3; ++j) {
        const char* pb = sb + b_rd + ks * 16 * G1_BSTR + j * 64;
        bfr[j] = cat44(tr_read(pb), tr_read(pb + 4 * G1_BSTR));
      }
#pragma unroll
      for (int j = 0; j < 3; ++j) acc[j] = MFMA32(bfr[j], af, acc[j]);
    }
    if (kt < 7) {
      char* sn = smem + ((kt + 1) & 1) * G1_BUF;
#pragma unroll
      for (int i = 0; i < 2; ++i) *(bf16x8*)(sn + a_loff[i]) = RA[(kt + 1) & 7][i];
#pragma unroll
      for (int i = 0; i < 3; ++i) *(bf16x8*)(sn + b_loff[i]) = RB[(kt + 1) & 7][i];
    }
    __syncthreads();
  }
#pragma unroll
  for (int j = 0; j < 3; ++j) {
    const int gc = gcol0 + 32 * j, sec = gc >> 9;
    const float sc = (sec == 0 && p) ? QSCALE : 1.f;
    u16* base = sec == 0 ? Qs : (sec == 1 ? Kp : Vt);
    const int s = mt * 128 + wm * 32 + r;
#pragma unroll
    for (int hd = 0; hd < 2; ++hd) {
      unsigned a0 = pk2(acc[j][8 * hd] * sc, acc[j][8 * hd + 1] * sc), a1 = pk2(acc[j][8 * hd + 2] * sc, acc[j][8 * hd + 3] * sc);
      unsigned b0 = pk2(acc[j][8 * hd + 4] * sc, acc[j][8 * hd + 5] * sc), b1 = pk2(acc[j][8 * hd + 6] * sc, acc[j][8 * hd + 7] * sc);
      auto r0 = __builtin_amdgcn_permlane32_swap(a0, b0, false, false);
      auto r1 = __builtin_amdgcn_permlane32_swap(a1, b1, false, false);
      u32x4 v = {r0[0], r1[0], r0[1], r1[1]};
      const int hh = ((gc & 511) >> 4) + hd;
      *(u32x4*)(base + ((size_t)(p * 32 + hh) * 2048 + s) * 16 + 8 * h) = v;
    }
  }
}

constexpr int G3_AS = 64 * 128;
constexpr int G3_BSTR = 320;
constexpr int G3_BS = 64 * G3_BSTR;
constexpr int G3_BUF = G3_AS + G3_BS;
constexpr int G3_LDS = 2 * G3_BUF;

__global__ __launch_bounds__(512) void out_gemm(const u16* __restrict__ Oc, const u16* __restrict__ woA,
                                                const u16* __restrict__ woB, u16* __restrict__ out) {
  extern __shared__ __attribute__((aligned(16))) char smem[];
  const int b = blockIdx.x, xg = b & 7, idx = b >> 3, p = xg & 1, mt = (xg >> 1) * 8 + (idx >> 2), nt = idx & 3;
  const u16* X = Oc + (size_t)p * 2048 * 512;
  const u16* W = p ? woB : woA;
  const int tid = threadIdx.x, w = __builtin_amdgcn_readfirstlane(tid >> 6), lane = tid & 63, r = lane & 31, h = lane >> 5;
  const int wm = w & 1, wn = w >> 1;
  int a_goff, a_loff, b_goff[2], b_loff[2];
  {
    int row = tid >> 3, ch = tid & 7;
    a_goff = (mt * 64 + row) * 512 + ch * 8;
    a_loff = row * 128 + ((ch ^ (row & 7)) << 4);
  }
#pragma unroll
  for (int i = 0; i < 2; ++i) {
    int c = tid + 512 * i, row = c >> 4, ch = c & 15;
    b_goff[i] = row * 512 + nt * 128 + ch * 8;
    b_loff[i] = G3_AS + row * G3_BSTR + ch * 16;
  }
  bf16x8 RA[8], RB[8][2];
#pragma unroll
  for (int kt = 0; kt < 8; ++kt) {
    RA[kt] = *(const bf16x8*)(X + a_goff + kt * 64);
#pragma unroll
    for (int i = 0; i < 2; ++i) RB[kt][i] = *(const bf16x8*)(W + b_goff[i] + kt * 64 * 512);
  }
  *(bf16x8*)(smem + a_loff) = RA[0];
#pragma unroll
  for (int i = 0; i < 2; ++i) *(bf16x8*)(smem + b_loff[i]) = RB[0][i];
  __syncthreads();
  f32x16 acc;
#pragma unroll
  for (int i = 0; i < 16; ++i) acc[i] = 0.f;
  const int q4 = (lane & 15) >> 2, p4 = lane & 3, blk = (lane >> 4) & 1;
  const int a_rd = (wm * 32 + r) * 128;
  const int b_rd = G3_AS + (8 * h + q4) * G3_BSTR + (wn * 32 + 16 * blk + 4 * p4) * 2;
#pragma unroll
  for (int kt = 0; kt < 8; ++kt) {
    const char* sb = smem + (kt & 1) * G3_BUF;
#pragma unroll
    for (int ks = 0; ks < 4; ++ks) {
      bf16x8 af = *(const bf16x8*)(sb + a_rd + (((2 * ks + h) ^ (r & 7)) << 4));
      const char* pb = sb + b_rd + ks * 16 * G3_BSTR;
      bf16x8 bfr = cat44(tr_read(pb), tr_read(pb + 4 * G3_BSTR));
      acc = MFMA32(bfr, af, acc);
    }
    if (kt < 7) {
      char* sn = smem + ((kt + 1) & 1) * G3_BUF;
      *(bf16x8*)(sn + a_loff) = RA[(kt + 1) & 7];
#pragma unroll
      for (int i = 0; i < 2; ++i) *(bf16x8*)(sn + b_loff[i]) = RB[(kt + 1) & 7][i];
    }
    __syncthreads();
  }
  const int s = mt * 64 + wm * 32 + r;
  u16* dst = out + (size_t)s * 1024 + p * 512 + nt * 128 + wn * 32 + 4 * h;
#pragma unroll
  for (int g = 0; g < 4; ++g) {
    u32x2 v = {pk2(acc[4 * g], acc[4 * g + 1]), pk2(acc[4 * g + 2], acc[4 * g + 3])};
    *(u32x2*)(dst + 8 * g) = v;
  }
}

constexpr int AT_VSTR = 4112;
constexpr int AT_K = 0, AT_V = 65536;
constexpr int AT_LDS = 163840;

DI float max16(const f32x16& s) {
  float a = fmaxf(fmaxf(s[0], s[1]), s[2]);
  float b = fmaxf(fmaxf(s[3], s[4]), s[5]);
  float c = fmaxf(fmaxf(s[6], s[7]), s[8]);
  float d = fmaxf(fmaxf(s[9], s[10]), s[11]);
  float e = fmaxf(fmaxf(s[12], s[13]), s[14]);
  a = fmaxf(fmaxf(a, b), c);
  d = fmaxf(fmaxf(d, e), s[15]);
  return fmaxf(a, d);
}

DI void attn_tile(f32x16 S, const bf16x8& vf0, const bf16x8& vf1, f32x16& negm, f32x16& O, bool first, f32x16& Sn) {
  float mx = max16(S);
  if (first || __any(mx > THR)) {
    float mo = __shfl_xor(mx, 32);
    float delta = fmaxf(mx, mo);
    if (!first) delta = fmaxf(delta, 0.f);
#pragma unroll
    for (int i = 0; i < 16; ++i) { S[i] -= delta; negm[i] -= delta; Sn[i] -= delta; }
    if (!first) {
      float alpha = __builtin_amdgcn_exp2f(-delta);
#pragma unroll
      for (int i = 0; i < 16; ++i) O[i] *= alpha;
    }
  }
#pragma unroll
  for (int i = 0; i < 16; ++i) S[i] = __builtin_amdgcn_exp2f(S[i]);
  u32x4 p0 = {pk2(S[0], S[1]), pk2(S[2], S[3]), pk2(S[4], S[5]), pk2(S[6], S[7])};
  u32x4 p1 = {pk2(S[8], S[9]), pk2(S[10], S[11]), pk2(S[12], S[13]), pk2(S[14], S[15])};
  O = MFMA32(vf0, __builtin_bit_cast(bf16x8, p0), O);
  O = MFMA32(vf1, __builtin_bit_cast(bf16x8, p1), O);
}

DI void attn_finish(const f32x16& O, u16* dst  , int r, int h) {
  float l = __shfl(O[8], r);
  float inv = 1.f / l;
  u32x2 v0 = {pk2(O[0] * inv, O[1] * inv), pk2(O[2] * inv, O[3] * inv)};
  u32x2 v1 = {pk2(O[4] * inv, O[5] * inv), pk2(O[6] * inv, O[7] * inv)};
  *(u32x2*)(dst + 4 * h) = v0;
  *(u32x2*)(dst + 8 + 4 * h) = v1;
}

constexpr int AT_KB = 0, AT_VB = 65536, AT_KA = 131072, AT_VA = 147456;
DI void glds16(const u16* g, char* l) {
  __builtin_amdgcn_global_load_lds((const void*)g, (__attribute__((address_space(3))) void*)l, 16, 0, 0);
}
DI void pv_tile(f32x16& S, const char* smem, int a0, int a1, int a2, int a3, f32x16& O) {
  bf16x8 v0 = cat44(tr_read(smem + a0), tr_read(smem + a1));
  bf16x8 v1 = cat44(tr_read(smem + a2), tr_read(smem + a3));
#pragma unroll
  for (int i = 0; i < 16; ++i) S[i] = __builtin_amdgcn_exp2f(S[i]);
  u32x4 p0 = {pk2(S[0], S[1]), pk2(S[2], S[3]), pk2(S[4], S[5]), pk2(S[6], S[7])};
  u32x4 p1 = {pk2(S[8], S[9]), pk2(S[10], S[11]), pk2(S[12], S[13]), pk2(S[14], S[15])};
  O = MFMA32(v0, __builtin_bit_cast(bf16x8, p0), O);
  O = MFMA32(v1, __builtin_bit_cast(bf16x8, p1), O);
}

__global__ __launch_bounds__(1024) void attn_kernel(const u16* __restrict__ Qs, const u16* __restrict__ Kp,
                                                   const u16* __restrict__ Vt, u16* __restrict__ Oc) {
  extern __shared__ __attribute__((aligned(16))) char smem[];
  const int b = blockIdx.x, xg = b & 7, idx = b >> 3;
  const int head = xg * 4 + (idx & 3), chunk = idx >> 2;
  const int tid = threadIdx.x, w16 = __builtin_amdgcn_readfirstlane(tid >> 6), lane = tid & 63, r = lane & 31, h = lane >> 5;
  const int w = w16 & 7, grp = w16 >> 3;
  const int q0 = chunk * 256 + w * 32;
  const int pr = (r & ~0xC) | ((r & 4) << 1) | ((r & 8) >> 1);
  const int kw0 = chunk * 256 - 256;
  const bf16x8 qfA = *(const bf16x8*)(Qs + ((size_t)head * 2048 + q0 + r) * 16 + 8 * h);
  const bf16x8 qfB = *(const bf16x8*)(Qs + ((size_t)(32 + head) * 2048 + q0 + r) * 16 + 8 * h);
  if (tid == 0) {
    u32x4 c0 = {0x00003f80u, 0u, 0u, 0u}, c1 = {0u, 0u, 0u, 0u};
    *(u32x4*)(smem + AT_VA) = c0;
    *(u32x4*)(smem + AT_VA + 16) = c1;
  }
  {
    const int pcA = (chunk > 0 || w16 >= 8) ? w16 : w16 + 8;
    const u16* KgA = Kp + ((size_t)head * 2048 + kw0) * 16;
    const u16* VgA = Vt + ((size_t)head * 2048 + kw0) * 16;
    glds16(KgA + pcA * 512 + lane * 8, smem + AT_KA + w16 * 1024);
    if (w16 != 0 || lane >= 2) glds16(VgA + pcA * 512 + lane * 8, smem + AT_VA + w16 * 1024);
    const u16* Kg = Kp + (size_t)(32 + head) * 2048 * 16 + lane * 8;
    const u16* Vg = Vt + (size_t)(32 + head) * 2048 * 16 + lane * 8;
    { const int t = 24 + w16; glds16(Kg + t * 512, smem + AT_KB + t * 1024); glds16(Vg + t * 512, smem + AT_VB + t * 1024); }
#pragma unroll
    for (int i = 0; i < 3; ++i) {
      const int pc = w16 * 3 + i, t = 40 + (pc >> 1);
      if (pc & 1) glds16(Vg + t * 512, smem + AT_VB + t * 1024); else glds16(Kg + t * 512, smem + AT_KB + t * 1024);
    }
#pragma unroll
    for (int i = 0; i < 3; ++i) {
      const int pc = w16 * 3 + i, t = (pc >> 1);
      if (pc & 1) glds16(Vg + t * 512, smem + AT_VB + t * 1024); else glds16(Kg + t * 512, smem + AT_KB + t * 1024);
    }
  }
#define STAGE(N) do { asm volatile("s_waitcnt vmcnt(" #N ") lgkmcnt(0)" ::: "memory"); __builtin_amdgcn_s_barrier(); asm volatile("" ::: "memory"); } while (0)
  STAGE(8);

  const int q4 = (lane & 15) >> 2, p4 = lane & 3, blk = (lane >> 4) & 1;
  const int vlane = blk ? (AT_VA + 8 * p4) : ((8 * h + q4) * 32 + 8 * p4);
  const int vinc = blk ? 0 : 1024;
  const int vo1 = blk ? 0 : 128, vo2 = blk ? 0 : 512, vo3 = blk ? 0 : 640;
  f32x16 negm, O, dz;
#pragma unroll
  for (int i = 0; i < 16; ++i) dz[i] = 0.f;

  if (grp == 0) {
    const int tmin = q0 >= 256 ? 0 : (256 - q0) >> 5;
    const int krow0 = w * 32;
    const char* kp = smem + AT_KA + (krow0 + pr) * 32 + 16 * h;
    float mx = -INFINITY;
#pragma unroll
    for (int t = 0; t < 9; ++t) {
      if (t >= tmin) {
        f32x16 S = MFMA32(*(const bf16x8*)(kp + t * 1024), qfA, dz);
#pragma unroll
        for (int i = 0; i < 16; ++i) {
          const int ko = (i & 3) + 4 * ((i >> 2) & 1) + 8 * h + 16 * (i >> 3);
          if (t == 8) S[i] = (ko <= r) ? S[i] : -INFINITY;
          if (t == 0) S[i] = (ko > r) ? S[i] : -INFINITY;
        }
        mx = fmaxf(mx, max16(S));
      }
    }
    STAGE(6);
    mx = fmaxf(mx, __shfl_xor(mx, 32));
    const float nm = -__uint_as_float(pk2(mx, mx) << 16) * QSCALE;
#pragma unroll
    for (int i = 0; i < 16; ++i) O[i] = 0.f;
#pragma unroll
    for (int t = 0; t < 9; ++t) {
      if (t >= tmin) {
        f32x16 S = MFMA32(*(const bf16x8*)(kp + t * 1024), qfA, dz);
#pragma unroll
        for (int i = 0; i < 16; i += 2) {
          const unsigned u = pk2(S[i], S[i + 1]);
          S[i] = fmaf(__uint_as_float(u << 16), QSCALE, nm);
          S[i + 1] = fmaf(__uint_as_float(u & 0xffff0000u), QSCALE, nm);
        }
#pragma unroll
        for (int i = 0; i < 16; ++i) {
          const int ko = (i & 3) + 4 * ((i >> 2) & 1) + 8 * h + 16 * (i >> 3);
          if (t == 8) S[i] = (ko <= r) ? S[i] : -INFINITY;
          if (t == 0) S[i] = (ko > r) ? S[i] : -INFINITY;
        }
        const int a = blk ? vlane : AT_VA + krow0 * 32 + t * 1024 + vlane;
        pv_tile(S, smem, a, a + vo1, a + vo2, a + vo3, O);
      }
    }
    attn_finish(O, Oc + (size_t)(q0 + r) * 512 + head * 16, r, h);
    STAGE(3);
    STAGE(0);
  } else {
    STAGE(6);
  }

  {
#pragma unroll
    for (int i = 0; i < 16; ++i) O[i] = 0.f;
    const char* kp = smem + AT_KB + pr * 32 + 16 * h;
    const int vb = blk ? vlane : AT_VB + vlane;
    {
      f32x16 S = MFMA32(*(const bf16x8*)(kp + 24 * 1024), qfB, dz);
      float mx = max16(S);
      mx = fmaxf(mx, __shfl_xor(mx, 32));
#pragma unroll
      for (int i = 0; i < 16; ++i) negm[i] = -mx;
      const int t0 = grp ? 24 : 0;
      int a = vb + t0 * vinc;
      const char* kq = kp + t0 * 1024;
#define RUN(NT) _Pragma("unroll 4") for (int t = 0; t < NT; ++t) { \
        S = MFMA32(*(const bf16x8*)(kq), qfB, negm); \
        pv_tile(S, smem, a, a + vo1, a + vo2, a + vo3, O); \
        a += vinc; kq += 1024; }
      if (grp) { RUN(16); STAGE(3); RUN(8); STAGE(0); RUN(16); }
      else { RUN(24); }
    }
    __syncthreads();
    float* mo = (float*)(smem + AT_KA + w * 2048) + lane;
    float* ml = (float*)(smem + AT_VA + 64 + w * 256) + lane;
    if (grp == 1) {
#pragma unroll
      for (int i = 0; i < 8; ++i) mo[i * 64] = O[i];
      *ml = O[8];
    }
    __syncthreads();
    if (grp == 1) return;
#pragma unroll
    for (int i = 0; i < 8; ++i) O[i] += mo[i * 64];
    O[8] += *ml;
    bool bad = false;
#pragma unroll
    for (int i = 0; i < 9; ++i) bad |= !(fabsf(O[i]) < 1e37f);
    if (__any(bad)) {
#pragma unroll
      for (int i = 0; i < 16; ++i) { negm[i] = 0.f; O[i] = 0.f; }
      f32x16 dummy;
      int a = vb;
      for (int t = 0; t < 64; ++t) {
        bf16x8 v0 = cat44(tr_read(smem + a), tr_read(smem + a + vo1));
        bf16x8 v1 = cat44(tr_read(smem + a + vo2), tr_read(smem + a + vo3));
        a += vinc;
        f32x16 S = MFMA32(*(const bf16x8*)(kp + t * 1024), qfB, negm);
        attn_tile(S, v0, v1, negm, O, t == 0, dummy);
      }
    }
    attn_finish(O, Oc + (size_t)(2048 + q0 + r) * 512 + head * 16, r, h);
  }
}

extern "C" void kernel_launch(void* const* d_in, const int* in_sizes, int n_in, void* d_out, int out_size, void* d_ws,
                              size_t ws_size, hipStream_t stream) {
  const u16* Ax = (const u16*)d_in[0];
  const u16* Awqkv = (const u16*)d_in[1];
  const u16* Awo = (const u16*)d_in[2];
  const u16* Bx = (const u16*)d_in[3];
  const u16* Bwqkv = (const u16*)d_in[4];
  const u16* Bwo = (const u16*)d_in[5];
  char* ws = (char*)d_ws;
  u16* Qs = (u16*)(ws);
  u16* Kp = (u16*)(ws + (4 << 20));
  u16* Vt = (u16*)(ws + (8 << 20));
  u16* Oc = (u16*)(ws + (12 << 20));
  (void)hipFuncSetAttribute((const void*)qkv_gemm, hipFuncAttributeMaxDynamicSharedMemorySize, G1_LDS);
  (void)hipFuncSetAttribute((const void*)attn_kernel, hipFuncAttributeMaxDynamicSharedMemorySize, AT_LDS);
  (void)hipFuncSetAttribute((const void*)out_gemm, hipFuncAttributeMaxDynamicSharedMemorySize, G3_LDS);
  qkv_gemm<<<256, 512, G1_LDS, stream>>>(Ax, Awqkv, Bx, Bwqkv, Qs, Kp, Vt);
  attn_kernel<<<256, 1024, AT_LDS, stream>>>(Qs, Kp, Vt, Oc);
  out_gemm<<<256, 512, G3_LDS, stream>>>(Oc, Awo, Bwo, (u16*)d_out);
}
```
